```python
import torch

def _softmax(x, axis=-1):
    return torch.softmax(x, dim=axis)

def _gelu(x):
    return torch.nn.functional.gelu(x, approximate='tanh')

def _silu(x):
    return torch.nn.functional.silu(x)

def _sigmoid(x):
    return torch.sigmoid(x)

def _layernorm(x, axis=-1, eps=1e-05):
    m = x.mean(dim=axis, keepdim=True)
    v = x.var(dim=axis, keepdim=True, unbiased=False)
    return (x - m) / torch.sqrt(v + eps)

def _scatter_add(tgt, idx, src, axis=0):
    return tgt.clone().index_add_(axis, idx, src)

def _segment_sum(src, seg, n):
    out = torch.zeros((n,) + src.shape[1:], dtype=src.dtype)
    return out.index_add_(0, seg, src)

def _pad(x, pw):
    flat = tuple((v for pair in reversed(pw) for v in pair))
    return torch.nn.functional.pad(x, flat)
kernel_name = 'adaLN_cond__grouped_kv_mqa__mixer_channel_mlp__prompt'

def setup_inputs():

    _w = lambda a: (lambda t: t.to(torch.bfloat16) if t.is_floating_point() else t)(torch.as_tensor(a))

    def _A_setup():
        B, S, D, H = (1, 2048, 640, 32)
        Dh = D // H
        return {'x': _w((lambda *s: torch.randn(*s, dtype=torch.bfloat16) * 0.05)(B, S, D)), 'wq': _w((lambda *s: torch.randn(*s, dtype=torch.bfloat16) * 0.05)(D, H * Dh)), 'wk': _w((lambda *s: torch.randn(*s, dtype=torch.bfloat16) * 0.05)(D, Dh)), 'wv': _w((lambda *s: torch.randn(*s, dtype=torch.bfloat16) * 0.05)(D, Dh)), 'wo': _w((lambda *s: torch.randn(*s, dtype=torch.bfloat16) * 0.05)(H * Dh, D))}
    _A_in = _A_setup()

    def _B_setup():
        B, S, D, F = (1, 2048, 640, 1728)
        return {'x': _w((lambda *s: torch.randn(*s, dtype=torch.bfloat16) * 0.05)(B, S, D)), 'w1': _w((lambda *s: torch.randn(*s, dtype=torch.bfloat16) * 3.2)(D, F) * D ** (-0.5)), 'b1': _w((lambda *s: torch.randn(*s, dtype=torch.bfloat16) * 0.8)(F)), 'w2': _w((lambda *s: torch.randn(*s, dtype=torch.bfloat16) * 3.2)(F, D) * F ** (-0.5)), 'b2': _w((lambda *s: torch.randn(*s, dtype=torch.bfloat16) * 3.2)(D))}
    _B_in = _B_setup()
    return {'A_x': _A_in['x'], 'A_wq': _A_in['wq'], 'A_wk': _A_in['wk'], 'A_wv': _A_in['wv'], 'A_wo': _A_in['wo'], 'B_x': _B_in['x'], 'B_w1': _B_in['w1'], 'B_b1': _B_in['b1'], 'B_w2': _B_in['w2'], 'B_b2': _B_in['b2'], 'c': _w((lambda *s: torch.randn(*s, dtype=torch.bfloat16) * 0.1)(1, 640) * 8.0), 'w_mod': _w((lambda *s: torch.randn(*s, dtype=torch.bfloat16) * 0.1)(640, 6 * 640) * 640 ** (-0.5) * 8.0)}

def reference(A_x, A_wq, A_wk, A_wv, A_wo, B_x, B_w1, B_b1, B_w2, B_b2, c, w_mod):

    def _A_ref(x, wq, wk, wv, wo):
        B, S, D = x.shape
        H = 32
        Dh = D // H
        q = (lambda x, *d: x.permute(*d))((x @ wq).reshape(B, S, H, Dh), 0, 2, 1, 3)
        k = x @ wk
        v = x @ wv
        scores = torch.einsum('bhqd,bkd->bhqk', q, k) * Dh ** (-0.5)
        p = _softmax(scores, axis=-1)
        o = torch.einsum('bhqk,bkd->bhqd', p, v)
        return (lambda x, *d: x.permute(*d))(o, 0, 2, 1, 3).reshape(B, S, H * Dh) @ wo

    def _B_ref(x, w1, b1, w2, b2):
        y = _layernorm(x, axis=-1)
        h = _gelu(torch.einsum('bsd,df->bsf', y, w1) + b1)
        return torch.einsum('bsf,fd->bsd', h, w2) + b2
    x = A_x
    mod = (_silu(c) @ w_mod).reshape(c.shape[0], 6, 1, -1)
    sa, ca, ga, sb, cb, gb = (mod[:, 0].clone(), mod[:, 1].clone(), mod[:, 2].clone(), mod[:, 3].clone(), mod[:, 4].clone(), mod[:, 5].clone())
    h = _layernorm(x, axis=-1) * (1 + ca) + sa
    x = x + ga * _A_ref(h, A_wq, A_wk, A_wv, A_wo)
    h = _layernorm(x, axis=-1) * (1 + cb) + sb
    x = x + gb * _B_ref(h, B_w1, B_b1, B_w2, B_b2)
    return x
```

```cpp
#include <hip/hip_runtime.h>
#include <hip/hip_bf16.h>
#include <stdint.h>

typedef __attribute__((ext_vector_type(8))) __bf16 bf16x8;
typedef __attribute__((ext_vector_type(2))) __bf16 bf16x2;
typedef __attribute__((ext_vector_type(4))) short s16x4;
typedef __attribute__((ext_vector_type(16))) float f32x16;
typedef __attribute__((ext_vector_type(4))) float f32x4;
typedef __attribute__((ext_vector_type(2))) float f32x2;
typedef __attribute__((ext_vector_type(4))) unsigned u32x4;
typedef __attribute__((ext_vector_type(2))) unsigned u32x2;
#define DI __device__ __forceinline__
#define LAS __attribute__((address_space(3)))

constexpr int S_ = 2048, D_ = 640, F_ = 1728;

constexpr size_t WS_MOD   = 0;
constexpr size_t WS_STAT  = 16384;
constexpr size_t WS_WQT   = 65536;
constexpr size_t WQT_SZ   = 34 * 25600;
constexpr size_t WS_WOT   = WS_WQT + 0x100000;
constexpr size_t WS_W1T   = WS_WOT + 0x100000;
constexpr size_t WS_W2T   = WS_W1T + 0x300000;
constexpr size_t WS_KV    = WS_W2T + 0x300000;
constexpr size_t WS_OP    = WS_KV + 0x100000;
constexpr size_t WS_X1    = WS_OP + 0x300000;
constexpr size_t WS_ST2   = WS_X1 + 0x300000;
constexpr size_t WS_HH    = WS_ST2 + 0x100000;
constexpr int KVT = 3072;
constexpr int KV_K1 = 1024, KV_ONE = 1280, KV_V = 1296;

struct Params {
  const uint16_t *x, *wq, *wk, *wv, *wo, *w1, *b1, *w2, *b2, *c, *wmod;
  uint16_t* out;
  unsigned char* ws;
};

DI float bf2f(uint16_t b) { return __builtin_bit_cast(float, (unsigned)b << 16); }
DI float bflo(unsigned u) { return __builtin_bit_cast(float, u << 16); }
DI float bfhi(unsigned u) { return __builtin_bit_cast(float, u & 0xffff0000u); }
DI unsigned pk2(float a, float b) { f32x2 v = {a, b}; return __builtin_bit_cast(unsigned, __builtin_convertvector(v, bf16x2)); }
DI float rbf(float a) { return bflo(pk2(a, 0.f)); }
DI f32x16 mfma32(u32x4 a, u32x4 b, f32x16 c) { return __builtin_amdgcn_mfma_f32_32x32x16_bf16(__builtin_bit_cast(bf16x8, a), __builtin_bit_cast(bf16x8, b), c, 0, 0, 0); }
DI f32x16 mfma32k8(u32x2 a, u32x2 b, f32x16 c) { return __builtin_amdgcn_mfma_f32_32x32x8bf16_1k(__builtin_bit_cast(s16x4, a), __builtin_bit_cast(s16x4, b), c, 0, 0, 0); }
DI f32x4 mfma16(u32x4 a, u32x4 b, f32x4 c) { return __builtin_amdgcn_mfma_f32_16x16x32_bf16(__builtin_bit_cast(bf16x8, a), __builtin_bit_cast(bf16x8, b), c, 0, 0, 0); }
DI float swap32(float v) {
  auto r = __builtin_amdgcn_permlane32_swap(__builtin_bit_cast(unsigned, v), __builtin_bit_cast(unsigned, v), false, false);
  return __builtin_bit_cast(float, (threadIdx.x & 32) ? r[0] : r[1]);
}

constexpr int NB_MOD = 120, NB_STAT = 128;
constexpr int IT_WQ = 80 * 340, IT_WO = 80 * 320, IT_W1 = 80 * 864, IT_W1Z = 64 * 80, IT_W2 = 224 * 320;
constexpr int IT_ALL = IT_WQ + IT_WO + IT_W1 + IT_W1Z + IT_W2;
constexpr int NB_RT = (IT_ALL + 255) / 256;

DI void colmap80(int nl, int& cb, int& row) {
  if (nl < 64) { int p = nl >> 5, rem = nl & 31, g = rem >> 3, e = rem & 7; cb = 2 * p + (e >> 2); row = 4 * g + (e & 3); }
  else { cb = 4; row = nl - 64; }
}
DI void tr_store(const unsigned (&v)[8], unsigned char* d0, unsigned char* d1) {
  u32x4 p0, p1;
#pragma unroll
  for (int i = 0; i < 4; ++i) {
    p0[i] = __builtin_amdgcn_perm(v[2 * i + 1], v[2 * i], 0x05040100u);
    p1[i] = __builtin_amdgcn_perm(v[2 * i + 1], v[2 * i], 0x07060302u);
  }
  *(u32x4*)d0 = p0; *(u32x4*)d1 = p1;
}

__global__ void __launch_bounds__(256) k_prep(Params p) {
  __shared__ float red[4][32];
  const int b = blockIdx.x, t = threadIdx.x;
  if (b < NB_MOD) {
    const int cl = t & 3, rg = t >> 2;
    const uint16_t* wp = p.wmod + b * 32 + cl * 8;
    u32x4 wv[10]; float sc[10];
#pragma unroll
    for (int i = 0; i < 10; ++i) wv[i] = *(const u32x4*)(wp + (size_t)(rg + 64 * i) * 3840);
#pragma unroll
    for (int i = 0; i < 10; ++i) { float c = bf2f(p.c[rg + 64 * i]); sc[i] = rbf(c / (1.f + __expf(-c))); }
    float acc[8];
#pragma unroll
    for (int e = 0; e < 8; ++e) acc[e] = 0.f;
#pragma unroll
    for (int i = 0; i < 10; ++i)
#pragma unroll
      for (int e = 0; e < 4; ++e) { acc[2 * e] += sc[i] * bflo(wv[i][e]); acc[2 * e + 1] += sc[i] * bfhi(wv[i][e]); }
#pragma unroll
    for (int e = 0; e < 8; ++e) {
#pragma unroll
      for (int m = 4; m < 64; m <<= 1) acc[e] += __shfl_xor(acc[e], m);
    }
    if ((t & 63) < 4) {
#pragma unroll
      for (int e = 0; e < 8; ++e) red[t >> 6][cl * 8 + e] = acc[e];
    }
    __syncthreads();
    if (t < 32) ((float*)(p.ws + WS_MOD))[b * 32 + t] = rbf(red[0][t] + red[1][t] + red[2][t] + red[3][t]);
    return;
  }
  if (b < NB_MOD + NB_STAT) {
    const int row = (b - NB_MOD) * 16 + (t >> 4), sl = t & 15;
    const uint16_t* xp = p.x + (size_t)row * D_ + sl * 8;
    u32x4 v[5];
#pragma unroll
    for (int i = 0; i < 5; ++i) v[i] = *(const u32x4*)(xp + i * 128);
    float s = 0.f;
#pragma unroll
    for (int i = 0; i < 5; ++i)
#pragma unroll
      for (int e = 0; e < 4; ++e) s += bflo(v[i][e]) + bfhi(v[i][e]);
#pragma unroll
    for (int m = 1; m < 16; m <<= 1) s += __shfl_xor(s, m);
    const float mu = s * (1.f / D_);
    float q = 0.f;
#pragma unroll
    for (int i = 0; i < 5; ++i)
#pragma unroll
      for (int e = 0; e < 4; ++e) { float a = bflo(v[i][e]) - mu, c = bfhi(v[i][e]) - mu; q += a * a + c * c; }
#pragma unroll
    for (int m = 1; m < 16; m <<= 1) q += __shfl_xor(q, m);
    if (sl == 0) { f32x2 st = {mu, 1.f / sqrtf(q * (1.f / D_) + 1e-5f)}; ((f32x2*)(p.ws + WS_STAT))[row] = st; }
    return;
  }
  int it = (b - NB_MOD - NB_STAT) * 256 + t;
  unsigned v[8];
  if (it < IT_WQ) {
    const int ko = it / 340, n = (it % 340) * 2, hd = n / 20, r = n % 20;
    const uint16_t* src; int ld;
    if (hd < 32) { src = p.wq + n; ld = 640; } else { src = (hd == 32 ? p.wk : p.wv) + r; ld = 20; }
#pragma unroll
    for (int jj = 0; jj < 8; ++jj) v[jj] = *(const unsigned*)(src + (size_t)(ko * 8 + jj) * ld);
    unsigned char* d = p.ws + WS_WQT + hd * 25600 + ko * 320 + r * 16;
    tr_store(v, d, d + 16);
    return;
  }
  it -= IT_WQ;
  if (it < IT_WO) {
    const int pc = it / 320, n = (it % 320) * 2, jw = pc / 10, q = pc % 10;
#pragma unroll
    for (int jj = 0; jj < 8; ++jj) {
      int k = q < 8 ? (4 * jw + (q >> 1)) * 20 + 4 * (q & 1) + (jj & 3) + 8 * (jj >> 2) : (4 * jw + 2 * (q - 8) + (jj >> 2)) * 20 + 16 + (jj & 3);
      v[jj] = *(const unsigned*)(p.wo + (size_t)k * 640 + n);
    }
    int cb, row; colmap80(n % 80, cb, row);
    unsigned char* d = p.ws + WS_WOT + (size_t)(n / 80) * 102400 + ((pc >> 2) * 5 + cb) * 1024 + ((pc & 3) * 16 + row) * 16;
    tr_store(v, d, d + 16);
    return;
  }
  it -= IT_WO;
  if (it < IT_W1) {
    const int ko = it / 864, f = (it % 864) * 2;
#pragma unroll
    for (int jj = 0; jj < 8; ++jj) v[jj] = *(const unsigned*)(p.w1 + (size_t)(ko * 8 + jj) * F_ + f);
    const int u0 = f / 27, l0 = f % 27, u1 = (f + 1) / 27, l1 = (f + 1) % 27;
    unsigned char* d0 = p.ws + WS_W1T + (size_t)u0 * 35840 + ko * 448 + (l0 < 23 ? l0 : l0 + 1) * 16;
    unsigned char* d1 = p.ws + WS_W1T + (size_t)u1 * 35840 + ko * 448 + (l1 < 23 ? l1 : l1 + 1) * 16;
    tr_store(v, d0, d1);
    return;
  }
  it -= IT_W1;
  if (it < IT_W1Z) {
    u32x4 z = {0u, 0u, 0u, 0u};
    *(u32x4*)(p.ws + WS_W1T + (size_t)(it / 80) * 35840 + (it % 80) * 448 + 23 * 16) = z;
    return;
  }
  it -= IT_W1Z;
  if (it < IT_W2) {
    const int gp = it / 320, n = (it % 320) * 2, w = gp / 28, pc = gp % 28;
#pragma unroll
    for (int jj = 0; jj < 8; ++jj) {
      int vv, rho;
      if (pc < 24) { vv = pc / 3; int a = pc % 3; rho = (a == 2 ? 16 : 4 * a) + (jj & 3) + 8 * (jj >> 2); }
      else { vv = 2 * (pc - 24) + (jj >> 2); rho = 20 + (jj & 3); }
      const int f = 216 * w + 27 * vv + (rho < 23 ? rho : rho - 1);
      unsigned val = *(const unsigned*)(p.w2 + (size_t)f * 640 + n);
      v[jj] = rho == 23 ? 0u : val;
    }
    int cb, row; colmap80(n % 80, cb, row);
    unsigned char* d = p.ws + WS_W2T + (size_t)((n / 80) * 8 + w) * 35840 + ((pc >> 2) * 5 + cb) * 1024 + ((pc & 3) * 16 + row) * 16;
    tr_store(v, d, d + 16);
  }
}

struct Geo { int tid, lane, w, tg, j, x; };
DI Geo geo() {
  Geo g; g.tid = threadIdx.x; g.lane = g.tid & 63; g.w = __builtin_amdgcn_readfirstlane(g.tid >> 6);
  const int b = blockIdx.x; g.x = b & 7; const int r = b >> 3; g.tg = g.x * 4 + (r >> 3); g.j = r & 7; return g;
}
constexpr int LDS_BYTES = 160 * 1024;
constexpr int L_VEC = 81920, L_RED = 90112;

template <bool TWO>
DI void build_panel(LAS unsigned char* L, const u32x4 (&xv)[10], const f32x4 (&sc)[2], int tid) {
  const int cg = tid & 15, ri = tid >> 4;
  const LAS f32x4* vg = (const LAS f32x4*)(L + L_VEC) + cg * 2;
#pragma unroll
  for (int c = 0; c < 5; ++c) {
    const f32x4 g0 = vg[c * 32], g1 = vg[c * 32 + 1], s0 = vg[160 + c * 32], s1 = vg[160 + c * 32 + 1];
    const int kp = cg + 16 * c;
#pragma unroll
    for (int i = 0; i < 2; ++i) {
      const u32x4 v = xv[c * 2 + i];
      float y[8];
#pragma unroll
      for (int e = 0; e < 4; ++e) { y[2 * e] = bflo(v[e]); y[2 * e + 1] = bfhi(v[e]); }
#pragma unroll
      for (int e = 0; e < 8; ++e) {
        const float g = e < 4 ? g0[e & 3] : g1[e & 3], s = e < 4 ? s0[e & 3] : s1[e & 3];
        const float n = __builtin_fmaf(y[e], sc[i][0], sc[i][1]);
        const float u = TWO ? __builtin_fmaf(s, sc[i][2], sc[i][3]) : s;
        y[e] = __builtin_fmaf(n, g, u);
      }
      u32x4 o = {pk2(y[0], y[1]), pk2(y[2], y[3]), pk2(y[4], y[5]), pk2(y[6], y[7])};
      *(LAS u32x4*)(L + i * 40960 + (kp >> 1) * 1024 + (kp & 1) * 512 + ((ri ^ (kp & 7)) * 16)) = o;
    }
  }
}
DI u32x4 panel_frag(LAS unsigned char* L, int tt, int ks, int lane) {
  const int c = lane & 31, h = lane >> 5;
  return *(const LAS u32x4*)(L + tt * 40960 + ks * 1024 + h * 512 + ((c ^ ((2 * ks + h) & 7)) * 16));
}

DI void p1_panel(const Params& p, LAS unsigned char* L, const Geo& G) {
  const int tid = G.tid, cg = tid & 15, ri = tid >> 4;
  u32x4 xv[10];
  const uint16_t* xp = p.x + (size_t)(G.tg * 64 + ri) * D_ + cg * 8;
#pragma unroll
  for (int c = 0; c < 5; ++c)
#pragma unroll
    for (int i = 0; i < 2; ++i) xv[c * 2 + i] = *(const u32x4*)(xp + i * 32 * D_ + c * 128);
  f32x4 sc[2];
#pragma unroll
  for (int i = 0; i < 2; ++i) { f32x2 st = ((const f32x2*)(p.ws + WS_STAT))[G.tg * 64 + ri + 32 * i]; sc[i] = f32x4{st[1], -st[0] * st[1], 0.f, 0.f}; }
  const float* mod = (const float*)(p.ws + WS_MOD);
  for (int i = tid; i < 640; i += 512) { ((LAS float*)(L + L_VEC))[i] = rbf(1.f + mod[640 + i]); ((LAS float*)(L + L_VEC))[640 + i] = mod[i]; }
  __syncthreads();
  build_panel<false>(L, xv, sc, tid);
  __syncthreads();
}

DI void p1_kv(const Params& p, LAS unsigned char* L, const Geo& G) {
  const int kind = G.j >> 1, tt = G.j & 1, lane = G.lane, r = lane & 31, h = lane >> 5;
  const unsigned char* wp = p.ws + WS_WQT + (32 + kind) * 25600 + (G.w * 5) * 640 + h * 320 + (r < 20 ? r : 19) * 16;
  f32x16 acc = {};
#pragma unroll
  for (int i = 0; i < 5; ++i) {
    const u32x4 wf = *(const u32x4*)(wp + i * 640);
    const u32x4 hf = panel_frag(L, tt, G.w * 5 + i, lane);
    acc = kind == 0 ? mfma32(wf, hf, acc) : mfma32(hf, wf, acc);
  }
  LAS f32x4* red = (LAS f32x4*)(L + L_RED);
#pragma unroll
  for (int q = 0; q < 4; ++q) red[(G.w * 4 + q) * 64 + lane] = f32x4{acc[4 * q], acc[4 * q + 1], acc[4 * q + 2], acc[4 * q + 3]};
  __syncthreads();
  if (G.w < 4) {
    const int q = G.w;
    f32x4 s = red[q * 64 + lane];
#pragma unroll
    for (int ww = 1; ww < 8; ++ww) s += red[(ww * 4 + q) * 64 + lane];
    u32x2 o = {pk2(s[0], s[1]), pk2(s[2], s[3])};
    unsigned char* tile = p.ws + WS_KV + (size_t)(G.tg * 2 + tt) * KVT;
    if (kind == 0) {
      if (q < 2) *(u32x2*)(tile + lane * 16 + q * 8) = o;
      else if (q == 2) {
        if (h == 0) *(u32x2*)(tile + KV_K1 + r * 8) = o;
        else if (r < 2) { u32x2 one = {r == 0 ? 0x3f80u : 0u, 0u}; *(u32x2*)(tile + KV_ONE + r * 8) = one; }
      }
    } else {
      if (r == 20) o = u32x2{0x3f803f80u, 0x3f803f80u};
      if (r <= 20) *(u32x2*)(tile + KV_V + ((q >> 1) * 2 + h) * 336 + r * 16 + (q & 1) * 8) = o;
    }
  }
}

DI void p1_q(const Params& p, LAS unsigned char* L, const Geo& G, u32x4& q0, u32x2& q1) {
  const int lane = G.lane, r = lane & 31, h = lane >> 5, hd = G.w & 3, th = G.w >> 2;
  const unsigned char* wp = p.ws + WS_WQT + (G.j * 4 + hd) * 25600 + (th * 20) * 640 + h * 320 + (r < 20 ? r : 19) * 16;
  f32x16 a0 = {}, a1 = {};
#pragma unroll
  for (int i = 0; i < 20; ++i) {
    const u32x4 wf = *(const u32x4*)(wp + i * 640);
    a0 = mfma32(wf, panel_frag(L, 0, th * 20 + i, lane), a0);
    a1 = mfma32(wf, panel_frag(L, 1, th * 20 + i, lane), a1);
  }
  LAS f32x4* red = (LAS f32x4*)(L + L_RED);
  __syncthreads();
  const f32x16 snd = th ? a0 : a1; f32x16 own = th ? a1 : a0;
#pragma unroll
  for (int q = 0; q < 3; ++q) red[(G.w * 3 + q) * 64 + lane] = f32x4{snd[4 * q], snd[4 * q + 1], snd[4 * q + 2], snd[4 * q + 3]};
  __syncthreads();
  const float sc = 0.22360679774997896f * 1.4426950408889634f;
#pragma unroll
  for (int q = 0; q < 3; ++q) { const f32x4 o = red[((G.w ^ 4) * 3 + q) * 64 + lane];
#pragma unroll
    for (int e = 0; e < 4; ++e) own[4 * q + e] = (own[4 * q + e] + o[e]) * sc; }
  q0 = u32x4{pk2(own[0], own[1]), pk2(own[2], own[3]), pk2(own[4], own[5]), pk2(own[6], own[7])};
  q1 = u32x2{pk2(own[8], own[9]), pk2(own[10], own[11])};
  if (h) q1 = u32x2{0u, 0u};
}

constexpr int CH = 8 * KVT;
DI void attn_slow(const Params& p, const Geo& G, u32x4 q0, u32x2 q1, f32x16& o) {
  const int lane = G.lane, r = lane & 31, h = lane >> 5;
  if (h) q1 = u32x2{0u, 0u};
  float m = -3.0e38f;
  o = f32x16{};
  const int vrow = r > 20 ? 20 : r;
  for (int t = 0; t < 64; ++t) {
    const unsigned char* tile = p.ws + WS_KV + (size_t)t * KVT;
    const u32x4 k0 = *(const u32x4*)(tile + lane * 16);
    const u32x2 k1 = *(const u32x2*)(tile + (h ? KV_ONE : KV_K1 + r * 8));
    f32x16 s = mfma32(k0, q0, f32x16{});
    s = mfma32k8(k1, q1, s);
    float mx = s[0];
#pragma unroll
    for (int i = 1; i < 16; ++i) mx = fmaxf(mx, s[i]);
    mx = fmaxf(mx, swap32(mx));
    const float mn = fmaxf(m, mx), al = __builtin_amdgcn_exp2f(m - mn);
    m = mn;
#pragma unroll
    for (int i = 0; i < 16; ++i) { o[i] *= al; s[i] = __builtin_amdgcn_exp2f(s[i] - mn); }
#pragma unroll
    for (int sI = 0; sI < 2; ++sI) {
      const u32x4 vf = *(const u32x4*)(tile + KV_V + (sI * 2 + h) * 336 + vrow * 16);
      const u32x4 pf = {pk2(s[8 * sI], s[8 * sI + 1]), pk2(s[8 * sI + 2], s[8 * sI + 3]), pk2(s[8 * sI + 4], s[8 * sI + 5]), pk2(s[8 * sI + 6], s[8 * sI + 7])};
      o = mfma32(vf, pf, o);
    }
  }
}

DI void p2_attn(const Params& p, LAS unsigned char* L, const Geo& G, u32x4 q0, u32x2 q1) {
  const int tid = G.tid, lane = G.lane, r = lane & 31, h = lane >> 5;
  const unsigned char* kv = p.ws + WS_KV + tid * 16;
  u32x4 st[3];
  int c = G.x;
#pragma unroll
  for (int i = 0; i < 3; ++i) st[i] = *(const u32x4*)(kv + c * CH + i * 8192);
#pragma unroll
  for (int i = 0; i < 3; ++i) *(LAS u32x4*)(L + tid * 16 + i * 8192) = st[i];
  __syncthreads();
  const LAS unsigned char* pk0 = L + lane * 16;
  const LAS unsigned char* pk1 = L + (h ? KV_ONE : KV_K1 + r * 8);
  const int vrow = r <= 20 ? r : (r < 28 ? 20 : 4);
  const LAS unsigned char* pv = L + KV_V + h * 336 + vrow * 16;
  {
    f32x16 s = mfma32(*(const LAS u32x4*)pk0, q0, f32x16{});
    s = mfma32k8(*(const LAS u32x2*)pk1, q1, s);
    float mx = s[0];
#pragma unroll
    for (int i = 1; i < 16; ++i) mx = fmaxf(mx, s[i]);
    mx = fmaxf(mx, swap32(mx));
    if (h) q1[0] = pk2(-mx, 0.f);
  }
  f32x16 o = {};
  for (int it = 0; it < 8; ++it) {
    const int buf = (it & 1) * CH;
    c = (c + 1) & 7;
    if (it < 7) {
#pragma unroll
      for (int i = 0; i < 3; ++i) st[i] = *(const u32x4*)(kv + c * CH + i * 8192);
    }
#pragma unroll
    for (int t = 0; t < 8; ++t) {
      const int off = buf + t * KVT;
      f32x16 s = mfma32(*(const LAS u32x4*)(pk0 + off), q0, f32x16{});
      s = mfma32k8(*(const LAS u32x2*)(pk1 + off), q1, s);
#pragma unroll
      for (int i = 0; i < 16; ++i) s[i] = __builtin_amdgcn_exp2f(s[i]);
#pragma unroll
      for (int sI = 0; sI < 2; ++sI) {
        const u32x4 vf = *(const LAS u32x4*)(pv + off + sI * 672);
        const u32x4 pf = {pk2(s[8 * sI], s[8 * sI + 1]), pk2(s[8 * sI + 2], s[8 * sI + 3]), pk2(s[8 * sI + 4], s[8 * sI + 5]), pk2(s[8 * sI + 6], s[8 * sI + 7])};
        o = mfma32(vf, pf, o);
      }
    }
    if (it < 7) {
#pragma unroll
      for (int i = 0; i < 3; ++i) *(LAS u32x4*)(L + (CH - buf) + tid * 16 + i * 8192) = st[i];
    }
    __syncthreads();
  }
  {
    float big = 0.f;
#pragma unroll
    for (int i = 0; i < 12; ++i) big = fmaxf(big, __builtin_fabsf(o[i]));
    bool bad = !(big < 1.0e30f);
#pragma unroll
    for (int i = 0; i < 12; ++i) bad = bad || (o[i] != o[i]);
    if (__any(bad)) attn_slow(p, G, q0, q1, o);
  }
  float l = o[8];
  { const float lo = swap32(l); l = h ? l : lo; }
  const float rl = 1.f / l;
  const int hd = G.w & 3, th = G.w >> 2, tok = th * 32 + r;
  unsigned char* op = p.ws + WS_OP + (size_t)(G.tg * 4 + (tok >> 4)) * 20480 + (tok & 15) * 16;
  const int pc = G.j * 10 + hd * 2 + h;
  *(u32x4*)(op + pc * 256) = u32x4{pk2(o[0] * rl, o[1] * rl), pk2(o[2] * rl, o[3] * rl), pk2(o[4] * rl, o[5] * rl), pk2(o[6] * rl, o[7] * rl)};
  if (!h) *(u32x2*)(op + (G.j * 10 + 8 + (hd >> 1)) * 256 + (hd & 1) * 8) = u32x2{pk2(o[8] * rl, o[9] * rl), pk2(o[10] * rl, o[11] * rl)};
}

DI void p3_oproj(const Params& p, LAS unsigned char* L, const Geo& G) {
  const int tid = G.tid, lane = G.lane, tb = G.w & 3, kh = G.w >> 2, tk = lane & 15, g = lane >> 4;
  for (int i = tid; i < 6400; i += 512) *(LAS u32x4*)(L + i * 16) = *(const u32x4*)(p.ws + WS_WOT + (size_t)G.j * 102400 + i * 16);
  const unsigned char* op = p.ws + WS_OP + (size_t)(G.tg * 4 + tb) * 20480 + kh * 10240 + lane * 16;
  u32x4 of[10];
#pragma unroll
  for (int i = 0; i < 10; ++i) of[i] = *(const u32x4*)(op + i * 1024);
  __syncthreads();
  f32x4 acc[5];
#pragma unroll
  for (int cb = 0; cb < 5; ++cb) acc[cb] = f32x4{};
#pragma unroll
  for (int i = 0; i < 10; ++i)
#pragma unroll
    for (int cb = 0; cb < 5; ++cb) acc[cb] = mfma16(*(const LAS u32x4*)(L + ((kh * 10 + i) * 5 + cb) * 1024 + lane * 16), of[i], acc[cb]);
  LAS f32x4* red = (LAS f32x4*)(L + 102400);
  if (kh == 0) { red[(tb * 5 + 2) * 64 + lane] = acc[2]; red[(tb * 5 + 3) * 64 + lane] = acc[3]; }
  else { red[(tb * 5 + 0) * 64 + lane] = acc[0]; red[(tb * 5 + 1) * 64 + lane] = acc[1]; red[(tb * 5 + 4) * 64 + lane] = acc[4]; }
  __syncthreads();
  float v[12];
  {
    const f32x4 a = (kh ? acc[2] : acc[0]) + red[(tb * 5 + 2 * kh) * 64 + lane], b = (kh ? acc[3] : acc[1]) + red[(tb * 5 + 2 * kh + 1) * 64 + lane];
    f32x4 c = acc[4]; if (!kh) c += red[(tb * 5 + 4) * 64 + lane];
#pragma unroll
    for (int e = 0; e < 4; ++e) { v[e] = a[e]; v[4 + e] = b[e]; v[8 + e] = c[e]; }
  }
  const int tok = G.tg * 64 + tb * 16 + tk, n0 = G.j * 80 + kh * 32 + g * 8, n1 = G.j * 80 + 64 + g * 4;
  const float* mod = (const float*)(p.ws + WS_MOD);
  const float mux = ((const f32x2*)(p.ws + WS_STAT))[tok][0];
  const u32x4 xa = *(const u32x4*)(p.x + (size_t)tok * D_ + n0);
  const u32x2 xb = *(const u32x2*)(p.x + (size_t)tok * D_ + n1);
  float s0 = 0.f, s1 = 0.f, s2 = 0.f, s3 = 0.f, s4 = 0.f, s5 = 0.f;
#pragma unroll
  for (int e = 0; e < 12; ++e) {
    const int n = e < 8 ? n0 + e : n1 + e - 8;
    const unsigned xw = e < 8 ? xa[e >> 1] : xb[(e - 8) >> 1];
    const float xx = (e & 1) ? bfhi(xw) : bflo(xw);
    const float x1 = rbf(xx + mod[1280 + n] * v[e]);
    v[e] = x1;
    const float u = (e >= 8 && kh) ? 0.f : x1 - mux, gg = rbf(1.f + mod[2560 + n]), sb = mod[1920 + n], ug = u * gg;
    s0 += u; s1 += u * u; s2 += ug; s3 += ug * gg; s4 += ug * ug; s5 += ug * sb;
  }
  uint16_t* x1p = (uint16_t*)(p.ws + WS_X1) + (size_t)tok * D_;
  *(u32x4*)(x1p + n0) = u32x4{pk2(v[0], v[1]), pk2(v[2], v[3]), pk2(v[4], v[5]), pk2(v[6], v[7])};
  if (!kh) *(u32x2*)(x1p + n1) = u32x2{pk2(v[8], v[9]), pk2(v[10], v[11])};
#pragma unroll
  for (int m = 16; m < 64; m <<= 1) { s0 += __shfl_xor(s0, m); s1 += __shfl_xor(s1, m); s2 += __shfl_xor(s2, m); s3 += __shfl_xor(s3, m); s4 += __shfl_xor(s4, m); s5 += __shfl_xor(s5, m); }
  LAS float* sred = (LAS float*)(L + 102400 + 20480);
  if (kh && g == 0) { LAS float* q = sred + (tb * 16 + tk) * 8; q[0] = s0; q[1] = s1; q[2] = s2; q[3] = s3; q[4] = s4; q[5] = s5; }
  __syncthreads();
  if (!kh && g == 0) {
    const LAS float* q = sred + (tb * 16 + tk) * 8;
    f32x4* d = (f32x4*)(p.ws + WS_ST2 + ((size_t)(G.tg * 8 + G.j) * 64 + tb * 16 + tk) * 32);
    d[0] = f32x4{s0 + q[0], s1 + q[1], s2 + q[2], s3 + q[3]};
    d[1] = f32x4{s4 + q[4], s5 + q[5], 0.f, 0.f};
  }
}

DI float gelu_t(float x) {
  const float u = x * __builtin_fmaf(x * x, 0.044715f, 1.f);
  const float e = __builtin_amdgcn_exp2f(u * (-2.f * 0.7978845608028654f * 1.4426950408889634f));
  return x * __builtin_amdgcn_rcpf(1.f + e);
}
constexpr int L_SCAL = 87040;
DI void p4_mlp1(const Params& p, LAS unsigned char* L, const Geo& G) {
  const int tid = G.tid, lane = G.lane, cg = tid & 15, ri = tid >> 4, r = lane & 31, h = lane >> 5;
  u32x4 xv[10];
  const uint16_t* xp = (const uint16_t*)(p.ws + WS_X1) + (size_t)(G.tg * 64 + ri) * D_ + cg * 8;
#pragma unroll
  for (int c = 0; c < 5; ++c)
#pragma unroll
    for (int i = 0; i < 2; ++i) xv[c * 2 + i] = *(const u32x4*)(xp + i * 32 * D_ + c * 128);
  const float* mod = (const float*)(p.ws + WS_MOD);
  for (int i = tid; i < 640; i += 512) { ((LAS float*)(L + L_VEC))[i] = rbf(1.f + mod[2560 + i]); ((LAS float*)(L + L_VEC))[640 + i] = mod[1920 + i]; }
  float cg_ = 0.f, cgg = 0.f, cgs = 0.f, cs = 0.f, css = 0.f;
#pragma unroll
  for (int i = 0; i < 10; ++i) { const float gg = rbf(1.f + mod[2560 + lane + 64 * i]), sb = mod[1920 + lane + 64 * i]; cg_ += gg; cgg += gg * gg; cgs += gg * sb; cs += sb; css += sb * sb; }
#pragma unroll
  for (int m = 1; m < 64; m <<= 1) { cg_ += __shfl_xor(cg_, m); cgg += __shfl_xor(cgg, m); cgs += __shfl_xor(cgs, m); cs += __shfl_xor(cs, m); css += __shfl_xor(css, m); }
  {
    const int tok = tid >> 3, jj = tid & 7;
    const f32x4* d = (const f32x4*)(p.ws + WS_ST2 + ((size_t)(G.tg * 8 + jj) * 64 + tok) * 32);
    f32x4 a = d[0], b = d[1];
#pragma unroll
    for (int m = 1; m < 8; m <<= 1) {
#pragma unroll
      for (int e = 0; e < 4; ++e) a[e] += __shfl_xor(a[e], m);
      b[0] += __shfl_xor(b[0], m); b[1] += __shfl_xor(b[1], m);
    }
    const float inv = 1.f / D_;
    const float mu = a[0] * inv;
    const float r1 = 1.f / sqrtf(fmaxf(a[1] * inv - mu * mu, 0.f) + 1e-5f);
    const float m2 = r1 * (a[2] - mu * cg_) * inv + cs * inv;
    const float e2 = r1 * r1 * (b[0] - 2.f * mu * a[3] + mu * mu * cgg) * inv + 2.f * r1 * (b[1] - mu * cgs) * inv + css * inv;
    const float r2 = 1.f / sqrtf(fmaxf(e2 - m2 * m2, 0.f) + 1e-5f);
    const float mux = ((const f32x2*)(p.ws + WS_STAT))[G.tg * 64 + tok][0];
    if (jj == 0) *(LAS f32x4*)(L + L_SCAL + tok * 16) = f32x4{r1 * r2, -(mu + mux) * r1 * r2, r2, -m2 * r2};
  }
  __syncthreads();
  f32x4 sc[2] = {*(const LAS f32x4*)(L + L_SCAL + ri * 16), *(const LAS f32x4*)(L + L_SCAL + (ri + 32) * 16)};
  build_panel<true>(L, xv, sc, tid);
  __syncthreads();
  const int u = G.j * 8 + G.w;
  const unsigned char* wp = p.ws + WS_W1T + (size_t)u * 35840 + h * 448 + (r < 28 ? r : 27) * 16;
  f32x16 a0 = {}, a1 = {};
#pragma unroll 8
  for (int ks = 0; ks < 40; ++ks) {
    const u32x4 wf = *(const u32x4*)(wp + ks * 896);
    a0 = mfma32(wf, panel_frag(L, 0, ks, lane), a0);
    a1 = mfma32(wf, panel_frag(L, 1, ks, lane), a1);
  }
  float bias[16];
#pragma unroll
  for (int i = 0; i < 16; ++i) {
    const int rho = (i & 3) + 8 * (i >> 2) + 4 * h;
    const int fl = rho < 23 ? rho : rho - 1;
    bias[i] = (rho == 23 || rho > 27) ? 0.f : bf2f(p.b1[27 * u + fl]);
  }
#pragma unroll
  for (int tt = 0; tt < 2; ++tt) {
    f32x16 a = tt ? a1 : a0;
#pragma unroll
    for (int i = 0; i < 16; ++i) a[i] = gelu_t(a[i] + bias[i]);
    const int tok = tt * 32 + r;
    unsigned char* hp = p.ws + WS_HH + (size_t)(G.tg * 4 + (tok >> 4)) * 57344 + G.j * 7168 + (tok & 15) * 16;
    *(u32x4*)(hp + (3 * G.w + h) * 256) = u32x4{pk2(a[0], a[1]), pk2(a[2], a[3]), pk2(a[4], a[5]), pk2(a[6], a[7])};
    if (!h) *(u32x4*)(hp + (3 * G.w + 2) * 256) = u32x4{pk2(a[8], a[9]), pk2(a[10], a[11]), pk2(a[12], a[13]), pk2(a[14], a[15])};
    else *(u32x2*)(hp + (24 + (G.w >> 1)) * 256 + (G.w & 1) * 8) = u32x2{pk2(a[8], a[9]), pk2(a[10], a[11])};
  }
}

DI void p5_mlp2(const Params& p, LAS unsigned char* L, const Geo& G) {
  const int lane = G.lane, w = G.w;
  const unsigned char* wp = p.ws + WS_W2T + (size_t)(G.j * 8 + w) * 35840 + lane * 16;
  u32x4 wf[35];
#pragma unroll
  for (int i = 0; i < 35; ++i) wf[i] = *(const u32x4*)(wp + i * 1024);
  const unsigned char* hp = p.ws + WS_HH + (size_t)(G.tg * 4) * 57344 + w * 7168 + lane * 16;
  LAS f32x4* red = (LAS f32x4*)L;
  const int ftb = w & 3, fkh = w >> 2;
  f32x4 keep[3] = {f32x4{}, f32x4{}, f32x4{}};
#pragma unroll
  for (int ps = 0; ps < 2; ++ps) {
    u32x4 hf[2][7];
#pragma unroll
    for (int a = 0; a < 2; ++a)
#pragma unroll
      for (int ks = 0; ks < 7; ++ks) hf[a][ks] = *(const u32x4*)(hp + (size_t)(ps * 2 + a) * 57344 + ks * 1024);
    f32x4 acc[2][5];
#pragma unroll
    for (int a = 0; a < 2; ++a)
#pragma unroll
      for (int cb = 0; cb < 5; ++cb) acc[a][cb] = f32x4{};
#pragma unroll
    for (int ks = 0; ks < 7; ++ks)
#pragma unroll
      for (int a = 0; a < 2; ++a)
#pragma unroll
        for (int cb = 0; cb < 5; ++cb) acc[a][cb] = mfma16(wf[ks * 5 + cb], hf[a][ks], acc[a][cb]);
#pragma unroll
    for (int a = 0; a < 2; ++a)
#pragma unroll
      for (int cb = 0; cb < 5; ++cb) {
        const int tb = ps * 2 + a, dst = tb + ((cb == 2 || cb == 3) ? 4 : 0);
        if (dst == w) keep[cb == 4 ? 2 : (cb & 1)] = acc[a][cb];
        else red[((tb * 5 + cb) * 7 + ((w - dst - 1) & 7)) * 64 + lane] = acc[a][cb];
      }
  }
  __syncthreads();
  float v[12];
#pragma unroll
  for (int q = 0; q < 3; ++q) {
    const int cb = q == 2 ? 4 : 2 * fkh + q;
    f32x4 s = keep[q];
#pragma unroll
    for (int i = 0; i < 7; ++i) s += red[((ftb * 5 + cb) * 7 + i) * 64 + lane];
#pragma unroll
    for (int e = 0; e < 4; ++e) v[4 * q + e] = s[e];
  }
  const int tk = lane & 15, g = lane >> 4;
  const int tok = G.tg * 64 + ftb * 16 + tk, n0 = G.j * 80 + fkh * 32 + g * 8, n1 = G.j * 80 + 64 + g * 4;
  const float* mod = (const float*)(p.ws + WS_MOD);
  const uint16_t* x1p = (const uint16_t*)(p.ws + WS_X1) + (size_t)tok * D_;
  const u32x4 xa = *(const u32x4*)(x1p + n0);
  const u32x2 xb = *(const u32x2*)(x1p + n1);
#pragma unroll
  for (int e = 0; e < 12; ++e) {
    const int n = e < 8 ? n0 + e : n1 + e - 8;
    const unsigned xw = e < 8 ? xa[e >> 1] : xb[(e - 8) >> 1];
    const float xx = (e & 1) ? bfhi(xw) : bflo(xw);
    v[e] = xx + mod[3200 + n] * (v[e] + bf2f(p.b2[n]));
  }
  uint16_t* op = p.out + (size_t)tok * D_;
  *(u32x4*)(op + n0) = u32x4{pk2(v[0], v[1]), pk2(v[2], v[3]), pk2(v[4], v[5]), pk2(v[6], v[7])};
  if (!fkh) *(u32x2*)(op + n1) = u32x2{pk2(v[8], v[9]), pk2(v[10], v[11])};
}

extern __shared__ __attribute__((aligned(16))) unsigned char smem[];
__global__ void __launch_bounds__(512, 2) k_kv(Params p) {
  LAS unsigned char* L = (LAS unsigned char*)smem; const Geo G = geo();
  if (G.j >= 4) return;
  p1_panel(p, L, G); p1_kv(p, L, G);
}
__global__ void __launch_bounds__(512, 2) k_attn(Params p) {
  LAS unsigned char* L = (LAS unsigned char*)smem; const Geo G = geo();
  p1_panel(p, L, G);
  u32x4 q0; u32x2 q1;
  p1_q(p, L, G, q0, q1);
  __syncthreads();
  p2_attn(p, L, G, q0, q1);
}
__global__ void __launch_bounds__(512, 2) k_oproj(Params p) { LAS unsigned char* L = (LAS unsigned char*)smem; const Geo G = geo(); p3_oproj(p, L, G); }
__global__ void __launch_bounds__(512, 2) k_mlp1(Params p) { LAS unsigned char* L = (LAS unsigned char*)smem; const Geo G = geo(); p4_mlp1(p, L, G); }
__global__ void __launch_bounds__(512, 2) k_mlp2(Params p) { LAS unsigned char* L = (LAS unsigned char*)smem; const Geo G = geo(); p5_mlp2(p, L, G); }

extern "C" void kernel_launch(void* const* d_in, const int* in_sizes, int n_in, void* d_out, int out_size, void* d_ws, size_t ws_size, hipStream_t stream) {
  Params p;
  p.x = (const uint16_t*)d_in[0]; p.wq = (const uint16_t*)d_in[1]; p.wk = (const uint16_t*)d_in[2]; p.wv = (const uint16_t*)d_in[3]; p.wo = (const uint16_t*)d_in[4];
  p.w1 = (const uint16_t*)d_in[6]; p.b1 = (const uint16_t*)d_in[7]; p.w2 = (const uint16_t*)d_in[8]; p.b2 = (const uint16_t*)d_in[9];
  p.c = (const uint16_t*)d_in[10]; p.wmod = (const uint16_t*)d_in[11];
  p.out = (uint16_t*)d_out; p.ws = (unsigned char*)d_ws;
  (void)hipFuncSetAttribute((const void*)k_kv, hipFuncAttributeMaxDynamicSharedMemorySize, LDS_BYTES);
  (void)hipFuncSetAttribute((const void*)k_attn, hipFuncAttributeMaxDynamicSharedMemorySize, LDS_BYTES);
  (void)hipFuncSetAttribute((const void*)k_oproj, hipFuncAttributeMaxDynamicSharedMemorySize, LDS_BYTES);
  (void)hipFuncSetAttribute((const void*)k_mlp1, hipFuncAttributeMaxDynamicSharedMemorySize, LDS_BYTES);
  (void)hipFuncSetAttribute((const void*)k_mlp2, hipFuncAttributeMaxDynamicSharedMemorySize, LDS_BYTES);
  k_prep<<<NB_MOD + NB_STAT + NB_RT, 256, 0, stream>>>(p);
  k_kv<<<256, 512, LDS_BYTES, stream>>>(p);
  k_attn<<<256, 512, LDS_BYTES, stream>>>(p);
  k_oproj<<<256, 512, LDS_BYTES, stream>>>(p);
  k_mlp1<<<256, 512, LDS_BYTES, stream>>>(p);
  k_mlp2<<<256, 512, LDS_BYTES, stream>>>(p);
}
```

```cpp
#include <hip/hip_runtime.h>
#include <hip/hip_bf16.h>
#include <stdint.h>

typedef __attribute__((ext_vector_type(8))) __bf16 bf16x8;
typedef __attribute__((ext_vector_type(2))) __bf16 bf16x2;
typedef __attribute__((ext_vector_type(4))) short s16x4;
typedef __attribute__((ext_vector_type(16))) float f32x16;
typedef __attribute__((ext_vector_type(4))) float f32x4;
typedef __attribute__((ext_vector_type(2))) float f32x2;
typedef __attribute__((ext_vector_type(4))) unsigned u32x4;
typedef __attribute__((ext_vector_type(2))) unsigned u32x2;
#define DI __device__ __forceinline__
#define LAS __attribute__((address_space(3)))

constexpr int S_ = 2048, D_ = 640, F_ = 1728;

constexpr size_t WS_MOD   = 0;
constexpr size_t WS_STAT  = 16384;
constexpr size_t WS_WQT   = 65536;
constexpr size_t WQT_SZ   = 34 * 25600;
constexpr size_t WS_WOT   = WS_WQT + 0x100000;
constexpr size_t WS_W1T   = WS_WOT + 0x100000;
constexpr size_t WS_W2T   = WS_W1T + 0x300000;
constexpr size_t WS_KV    = WS_W2T + 0x300000;
constexpr size_t WS_OP    = WS_KV + 0x100000;
constexpr size_t WS_X1    = WS_OP + 0x300000;
constexpr size_t WS_ST2   = WS_X1 + 0x300000;
constexpr size_t WS_HH    = WS_ST2 + 0x100000;
constexpr size_t WS_FLG   = 32768;
constexpr size_t WS_STAMP = WS_HH + 0x800000;
constexpr int FLG_GRP = 1024;
constexpr int FLG_WORDS = 1024 + 32 * 3 * 32;
constexpr int KVT = 3072;
constexpr int KV_K1 = 1024, KV_ONE = 1280, KV_V = 1296;

struct Params {
  const uint16_t *x, *wq, *wk, *wv, *wo, *w1, *b1, *w2, *b2, *c, *wmod;
  uint16_t* out;
  unsigned char* ws;
};

DI float bf2f(uint16_t b) { return __builtin_bit_cast(float, (unsigned)b << 16); }
DI float bflo(unsigned u) { return __builtin_bit_cast(float, u << 16); }
DI float bfhi(unsigned u) { return __builtin_bit_cast(float, u & 0xffff0000u); }
DI unsigned pk2(float a, float b) { f32x2 v = {a, b}; return __builtin_bit_cast(unsigned, __builtin_convertvector(v, bf16x2)); }
DI float rbf(float a) { return bflo(pk2(a, 0.f)); }
DI f32x16 mfma32(u32x4 a, u32x4 b, f32x16 c) { return __builtin_amdgcn_mfma_f32_32x32x16_bf16(__builtin_bit_cast(bf16x8, a), __builtin_bit_cast(bf16x8, b), c, 0, 0, 0); }
DI f32x16 mfma32k8(u32x2 a, u32x2 b, f32x16 c) { return __builtin_amdgcn_mfma_f32_32x32x8bf16_1k(__builtin_bit_cast(s16x4, a), __builtin_bit_cast(s16x4, b), c, 0, 0, 0); }
DI f32x4 mfma16(u32x4 a, u32x4 b, f32x4 c) { return __builtin_amdgcn_mfma_f32_16x16x32_bf16(__builtin_bit_cast(bf16x8, a), __builtin_bit_cast(bf16x8, b), c, 0, 0, 0); }
DI float swap32(float v) {
  auto r = __builtin_amdgcn_permlane32_swap(__builtin_bit_cast(unsigned, v), __builtin_bit_cast(unsigned, v), false, false);
  return __builtin_bit_cast(float, (threadIdx.x & 32) ? r[0] : r[1]);
}

constexpr int NB_MOD = 120, NB_STAT = 128;
constexpr int IT_WQ = 80 * 340, IT_WO = 80 * 320, IT_W1 = 80 * 864, IT_W1Z = 64 * 80, IT_W2 = 224 * 320;
constexpr int IT_ALL = IT_WQ + IT_WO + IT_W1 + IT_W1Z + IT_W2;
constexpr int NB_RT = (IT_ALL + 255) / 256;

DI void colmap80(int nl, int& cb, int& row) {
  if (nl < 64) { int p = nl >> 5, rem = nl & 31, g = rem >> 3, e = rem & 7; cb = 2 * p + (e >> 2); row = 4 * g + (e & 3); }
  else { cb = 4; row = nl - 64; }
}
DI void tr_store(const unsigned (&v)[8], unsigned char* d0, unsigned char* d1) {
  u32x4 p0, p1;
#pragma unroll
  for (int i = 0; i < 4; ++i) {
    p0[i] = __builtin_amdgcn_perm(v[2 * i + 1], v[2 * i], 0x05040100u);
    p1[i] = __builtin_amdgcn_perm(v[2 * i + 1], v[2 * i], 0x07060302u);
  }
  *(u32x4*)d0 = p0; *(u32x4*)d1 = p1;
}

__global__ void __launch_bounds__(256) k_prep(Params p) {
  __shared__ float red[4][32];
  const int b = blockIdx.x, t = threadIdx.x;
  if (b < NB_MOD) {
    const int cl = t & 3, rg = t >> 2;
    const uint16_t* wp = p.wmod + b * 32 + cl * 8;
    u32x4 wv[10]; float sc[10];
#pragma unroll
    for (int i = 0; i < 10; ++i) wv[i] = *(const u32x4*)(wp + (size_t)(rg + 64 * i) * 3840);
#pragma unroll
    for (int i = 0; i < 10; ++i) { float c = bf2f(p.c[rg + 64 * i]); sc[i] = rbf(c / (1.f + __expf(-c))); }
    float acc[8];
#pragma unroll
    for (int e = 0; e < 8; ++e) acc[e] = 0.f;
#pragma unroll
    for (int i = 0; i < 10; ++i)
#pragma unroll
      for (int e = 0; e < 4; ++e) { acc[2 * e] += sc[i] * bflo(wv[i][e]); acc[2 * e + 1] += sc[i] * bfhi(wv[i][e]); }
#pragma unroll
    for (int e = 0; e < 8; ++e) {
#pragma unroll
      for (int m = 4; m < 64; m <<= 1) acc[e] += __shfl_xor(acc[e], m);
    }
    if ((t & 63) < 4) {
#pragma unroll
      for (int e = 0; e < 8; ++e) red[t >> 6][cl * 8 + e] = acc[e];
    }
    __syncthreads();
    if (t < 32) ((float*)(p.ws + WS_MOD))[b * 32 + t] = rbf(red[0][t] + red[1][t] + red[2][t] + red[3][t]);
    return;
  }
  if (b < NB_MOD + NB_STAT) {
    const int row = (b - NB_MOD) * 16 + (t >> 4), sl = t & 15;
    const uint16_t* xp = p.x + (size_t)row * D_ + sl * 8;
    u32x4 v[5];
#pragma unroll
    for (int i = 0; i < 5; ++i) v[i] = *(const u32x4*)(xp + i * 128);
    float s = 0.f;
#pragma unroll
    for (int i = 0; i < 5; ++i)
#pragma unroll
      for (int e = 0; e < 4; ++e) s += bflo(v[i][e]) + bfhi(v[i][e]);
#pragma unroll
    for (int m = 1; m < 16; m <<= 1) s += __shfl_xor(s, m);
    const float mu = s * (1.f / D_);
    float q = 0.f;
#pragma unroll
    for (int i = 0; i < 5; ++i)
#pragma unroll
      for (int e = 0; e < 4; ++e) { float a = bflo(v[i][e]) - mu, c = bfhi(v[i][e]) - mu; q += a * a + c * c; }
#pragma unroll
    for (int m = 1; m < 16; m <<= 1) q += __shfl_xor(q, m);
    if (sl == 0) { f32x2 st = {mu, 1.f / sqrtf(q * (1.f / D_) + 1e-5f)}; ((f32x2*)(p.ws + WS_STAT))[row] = st; }
    if ((b - NB_MOD) * 256 + t < FLG_WORDS) ((unsigned*)(p.ws + WS_FLG))[(b - NB_MOD) * 256 + t] = 0u;
    return;
  }
  int it = (b - NB_MOD - NB_STAT) * 256 + t;
  unsigned v[8];
  if (it < IT_WQ) {
    const int ko = it / 340, n = (it % 340) * 2, hd = n / 20, r = n % 20;
    const uint16_t* src; int ld;
    if (hd < 32) { src = p.wq + n; ld = 640; } else { src = (hd == 32 ? p.wk : p.wv) + r; ld = 20; }
#pragma unroll
    for (int jj = 0; jj < 8; ++jj) v[jj] = *(const unsigned*)(src + (size_t)(ko * 8 + jj) * ld);
    unsigned char* d = p.ws + WS_WQT + hd * 25600 + ko * 320 + r * 16;
    tr_store(v, d, d + 16);
    return;
  }
  it -= IT_WQ;
  if (it < IT_WO) {
    const int pc = it / 320, n = (it % 320) * 2, jw = pc / 10, q = pc % 10;
#pragma unroll
    for (int jj = 0; jj < 8; ++jj) {
      int k = q < 8 ? (4 * jw + (q >> 1)) * 20 + 4 * (q & 1) + (jj & 3) + 8 * (jj >> 2) : (4 * jw + 2 * (q - 8) + (jj >> 2)) * 20 + 16 + (jj & 3);
      v[jj] = *(const unsigned*)(p.wo + (size_t)k * 640 + n);
    }
    int cb, row; colmap80(n % 80, cb, row);
    unsigned char* d = p.ws + WS_WOT + (size_t)(n / 80) * 102400 + ((pc >> 2) * 5 + cb) * 1024 + ((pc & 3) * 16 + row) * 16;
    tr_store(v, d, d + 16);
    return;
  }
  it -= IT_WO;
  if (it < IT_W1) {
    const int ko = it / 864, f = (it % 864) * 2;
#pragma unroll
    for (int jj = 0; jj < 8; ++jj) v[jj] = *(const unsigned*)(p.w1 + (size_t)(ko * 8 + jj) * F_ + f);
    const int u0 = f / 27, l0 = f % 27, u1 = (f + 1) / 27, l1 = (f + 1) % 27;
    unsigned char* d0 = p.ws + WS_W1T + (size_t)u0 * 35840 + ko * 448 + (l0 < 23 ? l0 : l0 + 1) * 16;
    unsigned char* d1 = p.ws + WS_W1T + (size_t)u1 * 35840 + ko * 448 + (l1 < 23 ? l1 : l1 + 1) * 16;
    tr_store(v, d0, d1);
    return;
  }
  it -= IT_W1;
  if (it < IT_W1Z) {
    u32x4 z = {0u, 0u, 0u, 0u};
    *(u32x4*)(p.ws + WS_W1T + (size_t)(it / 80) * 35840 + (it % 80) * 448 + 23 * 16) = z;
    return;
  }
  it -= IT_W1Z;
  if (it < IT_W2) {
    const int gp = it / 320, n = (it % 320) * 2, w = gp / 28, pc = gp % 28;
#pragma unroll
    for (int jj = 0; jj < 8; ++jj) {
      int vv, rho;
      if (pc < 24) { vv = pc / 3; int a = pc % 3; rho = (a == 2 ? 16 : 4 * a) + (jj & 3) + 8 * (jj >> 2); }
      else { vv = 2 * (pc - 24) + (jj >> 2); rho = 20 + (jj & 3); }
      const int f = 216 * w + 27 * vv + (rho < 23 ? rho : rho - 1);
      unsigned val = *(const unsigned*)(p.w2 + (size_t)f * 640 + n);
      v[jj] = rho == 23 ? 0u : val;
    }
    int cb, row; colmap80(n % 80, cb, row);
    unsigned char* d = p.ws + WS_W2T + (size_t)((n / 80) * 8 + w) * 35840 + ((pc >> 2) * 5 + cb) * 1024 + ((pc & 3) * 16 + row) * 16;
    tr_store(v, d, d + 16);
  }
}

#define GAS __attribute__((address_space(1)))
#define SBAR() __builtin_amdgcn_sched_barrier(0)
#define DRAIN() asm volatile("s_waitcnt vmcnt(0)" ::: "memory")
#define ORDER() __builtin_amdgcn_fence(__ATOMIC_ACQUIRE, "wavefront")
typedef __amdgpu_buffer_rsrc_t rsrc_t;
DI u32x4 ld16s(rsrc_t rs, int off) { return __builtin_amdgcn_raw_buffer_load_b128(rs, off, 0, 16); }
DI u32x2 ld8s(rsrc_t rs, int off) { return __builtin_amdgcn_raw_buffer_load_b64(rs, off, 0, 16); }
DI void st16s(rsrc_t rs, int off, u32x4 v) { __builtin_amdgcn_raw_buffer_store_b128(v, rs, off, 0, 16); }
DI void st8s(rsrc_t rs, int off, u32x2 v) { __builtin_amdgcn_raw_buffer_store_b64(v, rs, off, 0, 16); }
DI unsigned ldflag(const unsigned* p) { return __hip_atomic_load((const GAS unsigned*)p, __ATOMIC_RELAXED, __HIP_MEMORY_SCOPE_AGENT); }
DI void stflag(unsigned* p, unsigned v) { __hip_atomic_store((GAS unsigned*)p, v, __ATOMIC_RELAXED, __HIP_MEMORY_SCOPE_AGENT); }
DI void poll1(const unsigned* p) {
  for (int spins = 0; spins < (1 << 20); ++spins) {
    if (__all(ldflag(p) != 0u)) break;
    __builtin_amdgcn_s_sleep(1);
  }
  ORDER();
}
DI void poll2(const unsigned* p, const unsigned* q) {
  for (int spins = 0; spins < (1 << 20); ++spins) {
    const unsigned a = ldflag(p), b = ldflag(q);
    if (__all(a != 0u && b != 0u)) break;
    __builtin_amdgcn_s_sleep(1);
  }
  ORDER();
}
#define STAMP(i)

constexpr int LDS_BYTES = 160 * 1024;
constexpr int L_VEC = 81920;
constexpr int L_SCAL = 87040;
constexpr int L_RED = 90112;
constexpr int L_XCH = 122880;
constexpr int L_WO = 49152;
constexpr int L_R3 = 0;
constexpr int L_COL = 151552;
constexpr int L_B1 = 153152;
constexpr int CH = 8 * KVT;

template <bool TWO>
DI void build_panel(LAS unsigned char* L, const u32x4 (&xv)[10], const f32x4 (&sc)[2], int tid) {
  const int cg = tid & 15, ri = tid >> 4;
  const LAS f32x4* vg = (const LAS f32x4*)(L + L_VEC) + cg * 2;
#pragma unroll
  for (int c = 0; c < 5; ++c) {
    const f32x4 g0 = vg[c * 32], g1 = vg[c * 32 + 1], s0 = vg[160 + c * 32], s1 = vg[160 + c * 32 + 1];
    const int kp = cg + 16 * c;
#pragma unroll
    for (int i = 0; i < 2; ++i) {
      const u32x4 v = xv[c * 2 + i];
      float y[8];
#pragma unroll
      for (int e = 0; e < 4; ++e) { y[2 * e] = bflo(v[e]); y[2 * e + 1] = bfhi(v[e]); }
#pragma unroll
      for (int e = 0; e < 8; ++e) {
        const float g = e < 4 ? g0[e & 3] : g1[e & 3], s = e < 4 ? s0[e & 3] : s1[e & 3];
        const float n = __builtin_fmaf(y[e], sc[i][0], sc[i][1]);
        const float u = TWO ? __builtin_fmaf(s, sc[i][2], sc[i][3]) : s;
        y[e] = __builtin_fmaf(n, g, u);
      }
      u32x4 o = {pk2(y[0], y[1]), pk2(y[2], y[3]), pk2(y[4], y[5]), pk2(y[6], y[7])};
      *(LAS u32x4*)(L + i * 40960 + (kp >> 1) * 1024 + (kp & 1) * 512 + ((ri ^ (kp & 7)) * 16)) = o;
    }
  }
}
DI u32x4 panel_frag(LAS unsigned char* L, int tt, int ks, int lane) {
  const int c = lane & 31, h = lane >> 5;
  return *(const LAS u32x4*)(L + tt * 40960 + ks * 1024 + h * 512 + ((c ^ ((2 * ks + h) & 7)) * 16));
}

DI f32x16 attn_slow(rsrc_t rs, int lane, u32x4 q0, u32x2 q1) {
  const int r = lane & 31, h = lane >> 5;
  if (h) q1 = u32x2{0u, 0u};
  float m = -3.0e38f;
  f32x16 o = {};
  const int vrow = r > 20 ? 20 : r;
#pragma unroll 1
  for (int t = 0; t < 64; ++t) {
    const int tile = (int)WS_KV + t * KVT;
    const u32x4 k0 = ld16s(rs, tile + lane * 16);
    const u32x2 k1 = ld8s(rs, tile + (h ? KV_ONE : KV_K1 + r * 8));
    f32x16 s = mfma32(k0, q0, f32x16{});
    s = mfma32k8(k1, q1, s);
    float mx = s[0];
#pragma unroll
    for (int i = 1; i < 16; ++i) mx = fmaxf(mx, s[i]);
    mx = fmaxf(mx, swap32(mx));
    const float mn = fmaxf(m, mx), al = __builtin_amdgcn_exp2f(m - mn);
    m = mn;
#pragma unroll
    for (int i = 0; i < 16; ++i) { o[i] *= al; s[i] = __builtin_amdgcn_exp2f(s[i] - mn); }
#pragma unroll
    for (int sI = 0; sI < 2; ++sI) {
      const u32x4 vf = ld16s(rs, tile + KV_V + (sI * 2 + h) * 336 + vrow * 16);
      const u32x4 pf = {pk2(s[8 * sI], s[8 * sI + 1]), pk2(s[8 * sI + 2], s[8 * sI + 3]), pk2(s[8 * sI + 4], s[8 * sI + 5]), pk2(s[8 * sI + 6], s[8 * sI + 7])};
      o = mfma32(vf, pf, o);
    }
  }
  return o;
}

DI float gelu_t(float x) {
  const float u = x * __builtin_fmaf(x * x, 0.044715f, 1.f);
  const float e = __builtin_amdgcn_exp2f(u * (-2.f * 0.7978845608028654f * 1.4426950408889634f));
  return x * __builtin_amdgcn_rcpf(1.f + e);
}

constexpr int NPRE = 32;
extern __shared__ __attribute__((aligned(16))) unsigned char smem[];

__global__ void __launch_bounds__(512, 2) k_mega(Params p) {
  LAS unsigned char* L = (LAS unsigned char*)smem;
  const int tid = threadIdx.x, lane = tid & 63, w = __builtin_amdgcn_readfirstlane(tid >> 6);
  const int xl = blockIdx.x & 7, rk = blockIdx.x >> 3, tg = xl * 4 + (rk >> 3), j = rk & 7;
  const int r = lane & 31, h = lane >> 5, cg = tid & 15, ri = tid >> 4;
  const rsrc_t rs = __builtin_amdgcn_make_buffer_rsrc((void*)p.ws, 0, 0x7fffffff, 0x00020000);
  unsigned* flg = (unsigned*)(p.ws + WS_FLG);
  const float* mod = (const float*)(p.ws + WS_MOD);
  STAMP(0);
  u32x4 q0; u32x2 q1;
  {
    u32x4 xv[10];
    const uint16_t* xp = p.x + (size_t)(tg * 64 + ri) * D_ + cg * 8;
#pragma unroll
    for (int c = 0; c < 5; ++c)
#pragma unroll
      for (int i = 0; i < 2; ++i) xv[c * 2 + i] = *(const u32x4*)(xp + i * 32 * D_ + c * 128);
    f32x2 st[2];
#pragma unroll
    for (int i = 0; i < 2; ++i) st[i] = ((const f32x2*)(p.ws + WS_STAT))[tg * 64 + ri + 32 * i];
    f32x4 mv = {};
    if (tid < 320) mv = *(const f32x4*)(mod + tid * 4);
    else if (tid < 400) { const int a = (tid - 320) / 20, b = (tid - 320) % 20; mv = *(const f32x4*)(mod + (a == 0 ? 1280 : a == 1 ? 2560 : a == 2 ? 1920 : 3200) + j * 80 + b * 4); }
    else if (tid < 420) { const u32x2 bb = *(const u32x2*)(p.b2 + j * 80 + (tid - 400) * 4); mv = f32x4{bflo(bb[0]), bfhi(bb[0]), bflo(bb[1]), bfhi(bb[1])}; }
    float b1v = 0.f;
    if (tid >= 256) { const int rho = tid & 31, fl = rho < 23 ? rho : (rho > 27 ? 26 : rho - 1); b1v = bf2f(p.b1[216 * j + 27 * ((tid - 256) >> 5) + fl]); if (rho == 23) b1v = 0.f; }
    const int kind = j >> 1, tt = j & 1;
    u32x4 wkv[5];
    const int rc = (r < 20 ? r : 19) * 16 + h * 320;
    if (j < 4) {
      const unsigned char* wp = p.ws + WS_WQT + (32 + kind) * 25600 + (w * 5) * 640 + rc;
#pragma unroll
      for (int i = 0; i < 5; ++i) wkv[i] = *(const u32x4*)(wp + i * 640);
    }
    const int hd = w & 3, th = w >> 2;
    u32x4 wq[20];
    {
      const unsigned char* wp = p.ws + WS_WQT + (j * 4 + hd) * 25600 + (th * 20) * 640 + rc;
#pragma unroll
      for (int i = 0; i < 20; ++i) wq[i] = *(const u32x4*)(wp + i * 640);
    }
    SBAR();
    if (tid < 160) *(LAS f32x4*)(L + L_VEC + 2560 + tid * 16) = mv;
    else if (tid < 320) *(LAS f32x4*)(L + L_VEC + (tid - 160) * 16) = f32x4{rbf(1.f + mv[0]), rbf(1.f + mv[1]), rbf(1.f + mv[2]), rbf(1.f + mv[3])};
    else if (tid < 420) {
      if (tid >= 340 && tid < 360) mv = f32x4{rbf(1.f + mv[0]), rbf(1.f + mv[1]), rbf(1.f + mv[2]), rbf(1.f + mv[3])};
      *(LAS f32x4*)(L + L_COL + (tid - 320) * 16) = mv;
    }
    if (tid >= 256) *(LAS float*)(L + L_B1 + (tid - 256) * 4) = b1v;
    __syncthreads();
    STAMP(1);
    const f32x4 sc[2] = {f32x4{st[0][1], -st[0][0] * st[0][1], 0.f, 0.f}, f32x4{st[1][1], -st[1][0] * st[1][1], 0.f, 0.f}};
    build_panel<false>(L, xv, sc, tid);
    __syncthreads();
    STAMP(2);
    if (j < 4) {
      f32x16 acc = {};
#pragma unroll
      for (int i = 0; i < 5; ++i) {
        const u32x4 hf = panel_frag(L, tt, w * 5 + i, lane);
        acc = kind == 0 ? mfma32(wkv[i], hf, acc) : mfma32(hf, wkv[i], acc);
      }
      LAS f32x4* red = (LAS f32x4*)(L + L_RED);
#pragma unroll
      for (int q = 0; q < 4; ++q) red[(w * 4 + q) * 64 + lane] = f32x4{acc[4 * q], acc[4 * q + 1], acc[4 * q + 2], acc[4 * q + 3]};
      __syncthreads();
      if (w == 0) {
        unsigned o[8];
#pragma unroll
        for (int q = 0; q < 4; ++q) {
          f32x4 s = red[q * 64 + lane];
#pragma unroll
          for (int ww = 1; ww < 8; ++ww) s += red[(ww * 4 + q) * 64 + lane];
          o[2 * q] = pk2(s[0], s[1]); o[2 * q + 1] = pk2(s[2], s[3]);
        }
        const int tile = (int)WS_KV + (tg * 2 + tt) * KVT;
        if (kind == 0) {
          st16s(rs, tile + lane * 16, u32x4{o[0], o[1], o[2], o[3]});
          if (h == 0) st8s(rs, tile + KV_K1 + r * 8, u32x2{o[4], o[5]});
          else if (r < 2) st8s(rs, tile + KV_ONE + r * 8, u32x2{r == 0 ? 0x3f80u : 0u, 0u});
        } else if (r <= 20) {
          if (r == 20) {
#pragma unroll
            for (int q = 0; q < 8; ++q) o[q] = 0x3f803f80u;
          }
          st16s(rs, tile + KV_V + h * 336 + r * 16, u32x4{o[0], o[1], o[2], o[3]});
          st16s(rs, tile + KV_V + (2 + h) * 336 + r * 16, u32x4{o[4], o[5], o[6], o[7]});
        }
        DRAIN();
        if (lane < 8) stflag(flg + lane * 128 + tg * 4 + j, 1u);
      }
    }
    STAMP(3);
    f32x16 a0 = {}, a1 = {};
#pragma unroll
    for (int i = 0; i < 20; ++i) {
      a0 = mfma32(wq[i], panel_frag(L, 0, th * 20 + i, lane), a0);
      a1 = mfma32(wq[i], panel_frag(L, 1, th * 20 + i, lane), a1);
    }
    LAS f32x4* xch = (LAS f32x4*)(L + L_XCH);
    const f32x16 snd = th ? a0 : a1; f32x16 own = th ? a1 : a0;
#pragma unroll
    for (int q = 0; q < 3; ++q) xch[(w * 3 + q) * 64 + lane] = f32x4{snd[4 * q], snd[4 * q + 1], snd[4 * q + 2], snd[4 * q + 3]};
    STAMP(4);
    if (w == 7) poll2(flg + xl * 128 + lane, flg + xl * 128 + 64 + lane);
    __syncthreads();
    STAMP(5);
    const float qs = 0.22360679774997896f * 1.4426950408889634f;
#pragma unroll
    for (int q = 0; q < 3; ++q) { const f32x4 o = xch[((w ^ 4) * 3 + q) * 64 + lane];
#pragma unroll
      for (int e = 0; e < 4; ++e) own[4 * q + e] = (own[4 * q + e] + o[e]) * qs; }
    q0 = u32x4{pk2(own[0], own[1]), pk2(own[2], own[3]), pk2(own[4], own[5]), pk2(own[6], own[7])};
    q1 = u32x2{pk2(own[8], own[9]), pk2(own[10], own[11])};
    if (h) q1 = u32x2{0u, 0u};
  }
  u32x4 w1f[40];
  const unsigned char* w1p = p.ws + WS_W1T + (size_t)(j * 8 + w) * 35840 + h * 448 + (r < 28 ? r : 27) * 16;
  {
    const int kvo = (int)WS_KV + tid * 16;
    u32x4 st[3], wst[2];
#pragma unroll
    for (int i = 0; i < 3; ++i) st[i] = ld16s(rs, kvo + xl * CH + i * 8192);
#pragma unroll
    for (int i = 0; i < 3; ++i) *(LAS u32x4*)(L + tid * 16 + i * 8192) = st[i];
    __syncthreads();
    STAMP(6);
    const LAS unsigned char* pk0 = L + lane * 16;
    const LAS unsigned char* pk1 = L + (h ? KV_ONE : KV_K1 + r * 8);
    const int vrow = r <= 20 ? r : (r < 28 ? 20 : 4);
    const LAS unsigned char* pv = L + KV_V + h * 336 + vrow * 16;
    const unsigned char* wop = p.ws + WS_WOT + (size_t)j * 102400 + tid * 16;
    {
      f32x16 s = mfma32(*(const LAS u32x4*)pk0, q0, f32x16{});
      s = mfma32k8(*(const LAS u32x2*)pk1, q1, s);
      float mx = s[0];
#pragma unroll
      for (int i = 1; i < 16; ++i) mx = fmaxf(mx, s[i]);
      mx = fmaxf(mx, swap32(mx));
      if (h) q1[0] = pk2(-mx, 0.f);
    }
    f32x16 o = {};
#pragma unroll
    for (int it = 0; it < 8; ++it) {
      const int buf = (it & 1) * CH;
      if (it < 7) {
        const int c = (xl + it + 1) & 7;
#pragma unroll
        for (int i = 0; i < 3; ++i) st[i] = ld16s(rs, kvo + c * CH + i * 8192);
        wst[0] = *(const u32x4*)(wop + (2 * it) * 8192);
        if (it < 6 || tid < 256) wst[1] = *(const u32x4*)(wop + (it < 6 ? 2 * it + 1 : 2 * it) * 8192 + (it < 6 ? 0 : 0));
        if (it >= 1 && it <= 4) {
#pragma unroll
          for (int i = 0; i < NPRE / 4; ++i) w1f[(it - 1) * (NPRE / 4) + i] = *(const u32x4*)(w1p + ((it - 1) * (NPRE / 4) + i) * 896);
        }
        SBAR();
      }
#pragma unroll
      for (int t = 0; t < 8; ++t) {
        const int off = buf + t * KVT;
        f32x16 s = mfma32(*(const LAS u32x4*)(pk0 + off), q0, f32x16{});
        s = mfma32k8(*(const LAS u32x2*)(pk1 + off), q1, s);
#pragma unroll
        for (int i = 0; i < 16; ++i) s[i] = __builtin_amdgcn_exp2f(s[i]);
#pragma unroll
        for (int sI = 0; sI < 2; ++sI) {
          const u32x4 vf = *(const LAS u32x4*)(pv + off + sI * 672);
          const u32x4 pf = {pk2(s[8 * sI], s[8 * sI + 1]), pk2(s[8 * sI + 2], s[8 * sI + 3]), pk2(s[8 * sI + 4], s[8 * sI + 5]), pk2(s[8 * sI + 6], s[8 * sI + 7])};
          o = mfma32(vf, pf, o);
        }
      }
      if (it < 7) {
        SBAR();
#pragma unroll
        for (int i = 0; i < 3; ++i) *(LAS u32x4*)(L + (CH - buf) + tid * 16 + i * 8192) = st[i];
        if (it < 6 || tid < 256) *(LAS u32x4*)(L + L_WO + (2 * it) * 8192 + tid * 16) = wst[0];
        if (it < 6) *(LAS u32x4*)(L + L_WO + (2 * it + 1) * 8192 + tid * 16) = wst[1];
        __syncthreads();
      }
    }
    STAMP(7);
    {
      float big = 0.f;
#pragma unroll
      for (int i = 0; i < 12; ++i) big = fmaxf(big, __builtin_fabsf(o[i]));
      bool bad = !(big < 1.0e30f);
#pragma unroll
      for (int i = 0; i < 12; ++i) bad = bad || (o[i] != o[i]);
      if (__any(bad)) o = attn_slow(rs, lane, q0, q1);
    }
    float l = o[8];
    { const float lo = swap32(l); l = h ? l : lo; }
    const float rl = 1.f / l;
    const int hd = w & 3, th = w >> 2, tok = th * 32 + r;
    const int op = (int)WS_OP + (tg * 4 + (tok >> 4)) * 20480 + (tok & 15) * 16;
    st16s(rs, op + (j * 10 + hd * 2 + h) * 256, u32x4{pk2(o[0] * rl, o[1] * rl), pk2(o[2] * rl, o[3] * rl), pk2(o[4] * rl, o[5] * rl), pk2(o[6] * rl, o[7] * rl)});
    if (!h) st8s(rs, op + (j * 10 + 8 + (hd >> 1)) * 256 + (hd & 1) * 8, u32x2{pk2(o[8] * rl, o[9] * rl), pk2(o[10] * rl, o[11] * rl)});
    DRAIN();
    __syncthreads();
    if (tid == 0) stflag(flg + FLG_GRP + (tg * 3 + 0) * 32 + j, 1u);
    STAMP(8);
  }
  const int tb = w & 3, kh = w >> 2, tk = lane & 15, g = lane >> 4;
  const int tok3 = tg * 64 + tb * 16 + tk, c0 = kh * 32 + g * 8, c1 = 64 + g * 4;
  u32x4 x1a; u32x2 x1b;
  {
    const u32x4 xa = *(const u32x4*)(p.x + (size_t)tok3 * D_ + j * 80 + c0);
    const u32x2 xb = *(const u32x2*)(p.x + (size_t)tok3 * D_ + j * 80 + c1);
    const float mux = ((const f32x2*)(p.ws + WS_STAT))[tok3][0];
    poll1(flg + FLG_GRP + (tg * 3 + 0) * 32 + kh * 4 + (lane & 3));
    STAMP(9);
    const int op = (int)WS_OP + (tg * 4 + tb) * 20480 + kh * 10240 + lane * 16;
    u32x4 of[10];
#pragma unroll
    for (int i = 0; i < 10; ++i) of[i] = ld16s(rs, op + i * 1024);
    SBAR();
    f32x4 acc[5];
#pragma unroll
    for (int cb = 0; cb < 5; ++cb) acc[cb] = f32x4{};
#pragma unroll
    for (int i = 0; i < 10; ++i)
#pragma unroll
      for (int cb = 0; cb < 5; ++cb) acc[cb] = mfma16(*(const LAS u32x4*)(L + L_WO + ((kh * 10 + i) * 5 + cb) * 1024 + lane * 16), of[i], acc[cb]);
    LAS f32x4* red = (LAS f32x4*)(L + L_R3);
    if (kh == 0) { red[(tb * 5 + 2) * 64 + lane] = acc[2]; red[(tb * 5 + 3) * 64 + lane] = acc[3]; }
    else { red[(tb * 5 + 0) * 64 + lane] = acc[0]; red[(tb * 5 + 1) * 64 + lane] = acc[1]; red[(tb * 5 + 4) * 64 + lane] = acc[4]; }
    __syncthreads();
    STAMP(10);
    float v[12];
    {
      const f32x4 a = (kh ? acc[2] : acc[0]) + red[(tb * 5 + 2 * kh) * 64 + lane], b = (kh ? acc[3] : acc[1]) + red[(tb * 5 + 2 * kh + 1) * 64 + lane];
      const f32x4 c = acc[4] + red[(tb * 5 + 4) * 64 + lane];
#pragma unroll
      for (int e = 0; e < 4; ++e) { v[e] = a[e]; v[4 + e] = b[e]; v[8 + e] = c[e]; }
    }
    const LAS float* col = (const LAS float*)(L + L_COL);
    float s0 = 0.f, s1 = 0.f, s2 = 0.f, s3 = 0.f, s4 = 0.f, s5 = 0.f;
#pragma unroll
    for (int q = 0; q < 3; ++q) {
      const int n = q < 2 ? c0 + 4 * q : c1;
      const f32x4 ga = *(const LAS f32x4*)(col + n), g2 = *(const LAS f32x4*)(col + 80 + n), sb = *(const LAS f32x4*)(col + 160 + n);
#pragma unroll
      for (int e = 0; e < 4; ++e) {
        const unsigned xw = q < 2 ? xa[2 * q + (e >> 1)] : xb[e >> 1];
        const float xx = (e & 1) ? bfhi(xw) : bflo(xw);
        const float x1 = rbf(xx + ga[e] * v[4 * q + e]);
        v[4 * q + e] = x1;
        const float u = (q == 2 && kh) ? 0.f : x1 - mux, ug = u * g2[e];
        s0 += u; s1 += u * u; s2 += ug; s3 += ug * g2[e]; s4 += ug * ug; s5 += ug * sb[e];
      }
    }
    x1a = u32x4{pk2(v[0], v[1]), pk2(v[2], v[3]), pk2(v[4], v[5]), pk2(v[6], v[7])};
    x1b = u32x2{pk2(v[8], v[9]), pk2(v[10], v[11])};
    const int x1o = (int)WS_X1 + (tok3 * D_ + j * 80) * 2;
    st16s(rs, x1o + c0 * 2, x1a);
    if (!kh) st8s(rs, x1o + c1 * 2, x1b);
#pragma unroll
    for (int m = 16; m < 64; m <<= 1) { s0 += __shfl_xor(s0, m); s1 += __shfl_xor(s1, m); s2 += __shfl_xor(s2, m); s3 += __shfl_xor(s3, m); s4 += __shfl_xor(s4, m); s5 += __shfl_xor(s5, m); }
    if (g < 2) {
      const f32x4 sv = g == 0 ? f32x4{s0, s1, s2, s3} : f32x4{s4, s5, 0.f, 0.f};
      st16s(rs, (int)WS_ST2 + ((tok3 * 16 + j * 2 + kh) * 2 + g) * 16, __builtin_bit_cast(u32x4, sv));
    }
    DRAIN();
    __syncthreads();
    if (tid == 0) stflag(flg + FLG_GRP + (tg * 3 + 1) * 32 + j, 1u);
    STAMP(11);
  }
  u32x4 w2f[35];
  {
    f32x4 mv = {};
    if (tid < 320) mv = *(const f32x4*)(mod + 1920 + tid * 4);
    const float mux = ((const f32x2*)(p.ws + WS_STAT))[tg * 64 + (tid >> 3)][0];
    if (tid < 160) *(LAS f32x4*)(L + L_VEC + 2560 + tid * 16) = mv;
    else if (tid < 320) *(LAS f32x4*)(L + L_VEC + (tid - 160) * 16) = f32x4{rbf(1.f + mv[0]), rbf(1.f + mv[1]), rbf(1.f + mv[2]), rbf(1.f + mv[3])};
    __syncthreads();
    float cg_ = 0.f, cgg = 0.f, cgs = 0.f, cs = 0.f, css = 0.f;
#pragma unroll
    for (int i = 0; i < 10; ++i) { const float gg = ((const LAS float*)(L + L_VEC))[lane + 64 * i], sb = ((const LAS float*)(L + L_VEC))[640 + lane + 64 * i]; cg_ += gg; cgg += gg * gg; cgs += gg * sb; cs += sb; css += sb * sb; }
#pragma unroll
    for (int m = 1; m < 64; m <<= 1) { cg_ += __shfl_xor(cg_, m); cgg += __shfl_xor(cgg, m); cgs += __shfl_xor(cgs, m); cs += __shfl_xor(cs, m); css += __shfl_xor(css, m); }
    poll1(flg + FLG_GRP + (tg * 3 + 1) * 32 + (lane & 7));
    STAMP(12);
    f32x4 a, b;
    {
      const int so = (int)WS_ST2 + ((tg * 64 + (tid >> 3)) * 16 + (tid & 7) * 2) * 32;
      const u32x4 t0 = ld16s(rs, so), t1 = ld16s(rs, so + 16), t2 = ld16s(rs, so + 32), t3 = ld16s(rs, so + 48);
      u32x4 xv[10];
      const int xo = (int)WS_X1 + ((tg * 64 + ri) * D_ + cg * 8) * 2;
#pragma unroll
      for (int c = 0; c < 5; ++c)
#pragma unroll
        for (int i = 0; i < 2; ++i) xv[c * 2 + i] = ld16s(rs, xo + (i * 32 * D_ + c * 128) * 2);
      SBAR();
      a = __builtin_bit_cast(f32x4, t0) + __builtin_bit_cast(f32x4, t2); b = __builtin_bit_cast(f32x4, t1) + __builtin_bit_cast(f32x4, t3);
#pragma unroll
      for (int m = 1; m < 8; m <<= 1) {
#pragma unroll
        for (int e = 0; e < 4; ++e) a[e] += __shfl_xor(a[e], m);
        b[0] += __shfl_xor(b[0], m); b[1] += __shfl_xor(b[1], m);
      }
      const float inv = 1.f / D_;
      const float mu = a[0] * inv;
      const float r1 = 1.f / sqrtf(fmaxf(a[1] * inv - mu * mu, 0.f) + 1e-5f);
      const float m2 = r1 * (a[2] - mu * cg_) * inv + cs * inv;
      const float e2 = r1 * r1 * (b[0] - 2.f * mu * a[3] + mu * mu * cgg) * inv + 2.f * r1 * (b[1] - mu * cgs) * inv + css * inv;
      const float r2 = 1.f / sqrtf(fmaxf(e2 - m2 * m2, 0.f) + 1e-5f);
      if ((tid & 7) == 0) *(LAS f32x4*)(L + L_SCAL + (tid >> 3) * 16) = f32x4{r1 * r2, -(mu + mux) * r1 * r2, r2, -m2 * r2};
      __syncthreads();
      STAMP(13);
      const f32x4 sc[2] = {*(const LAS f32x4*)(L + L_SCAL + ri * 16), *(const LAS f32x4*)(L + L_SCAL + (ri + 32) * 16)};
      build_panel<true>(L, xv, sc, tid);
    }
    SBAR();
#pragma unroll
    for (int i = NPRE; i < 40; ++i) w1f[i] = *(const u32x4*)(w1p + i * 896);
    __syncthreads();
    STAMP(14);
    const unsigned char* w2p = p.ws + WS_W2T + (size_t)(j * 8 + w) * 35840 + lane * 16;
    f32x16 a0;
#pragma unroll
    for (int q = 0; q < 4; ++q) { const f32x4 bb = *(const LAS f32x4*)(L + L_B1 + w * 128 + q * 32 + h * 16);
#pragma unroll
      for (int e = 0; e < 4; ++e) a0[4 * q + e] = bb[e]; }
    f32x16 a1 = a0;
#pragma unroll
    for (int ks = 0; ks < 40; ++ks) {
      a0 = mfma32(w1f[ks], panel_frag(L, 0, ks, lane), a0);
      a1 = mfma32(w1f[ks], panel_frag(L, 1, ks, lane), a1);
      if (ks >= 5) { w2f[ks - 5] = *(const u32x4*)(w2p + (ks - 5) * 1024); }
      if ((ks & 3) == 3) SBAR();
    }
    STAMP(15);
#pragma unroll
    for (int tt = 0; tt < 2; ++tt) {
      f32x16 aa = tt ? a1 : a0;
#pragma unroll
      for (int i = 0; i < 16; ++i) aa[i] = gelu_t(aa[i]);
      const int tok = tt * 32 + r;
      const int hp = (int)WS_HH + (tg * 4 + (tok >> 4)) * 57344 + j * 7168 + (tok & 15) * 16;
      st16s(rs, hp + (3 * w + h) * 256, u32x4{pk2(aa[0], aa[1]), pk2(aa[2], aa[3]), pk2(aa[4], aa[5]), pk2(aa[6], aa[7])});
      if (!h) st16s(rs, hp + (3 * w + 2) * 256, u32x4{pk2(aa[8], aa[9]), pk2(aa[10], aa[11]), pk2(aa[12], aa[13]), pk2(aa[14], aa[15])});
      else st8s(rs, hp + (24 + (w >> 1)) * 256 + (w & 1) * 8, u32x2{pk2(aa[8], aa[9]), pk2(aa[10], aa[11])});
    }
    DRAIN();
    __syncthreads();
    if (tid == 0) stflag(flg + FLG_GRP + (tg * 3 + 2) * 32 + j, 1u);
    STAMP(16);
  }
  {
    poll1(flg + FLG_GRP + (tg * 3 + 2) * 32 + w);
    STAMP(17);
    const int hp = (int)WS_HH + (tg * 4) * 57344 + w * 7168 + lane * 16;
    LAS f32x4* red = (LAS f32x4*)L;
    f32x4 keep[3] = {f32x4{}, f32x4{}, f32x4{}};
    u32x4 hf[2][7];
#pragma unroll
    for (int a = 0; a < 2; ++a)
#pragma unroll
      for (int ks = 0; ks < 7; ++ks) hf[a][ks] = ld16s(rs, hp + a * 57344 + ks * 1024);
    SBAR();
#pragma unroll
    for (int tq = 0; tq < 4; ++tq) {
      f32x4 acc[5];
#pragma unroll
      for (int cb = 0; cb < 5; ++cb) acc[cb] = f32x4{};
#pragma unroll
      for (int ks = 0; ks < 7; ++ks) {
#pragma unroll
        for (int cb = 0; cb < 5; ++cb) acc[cb] = mfma16(w2f[ks * 5 + cb], hf[tq & 1][ks], acc[cb]);
        if (tq < 2) hf[tq & 1][ks] = ld16s(rs, hp + (tq + 2) * 57344 + ks * 1024);
        SBAR();
      }
#pragma unroll
      for (int cb = 0; cb < 5; ++cb) {
        const int dst = tq + ((cb == 2 || cb == 3) ? 4 : 0);
        if (dst == w) keep[cb == 4 ? 2 : (cb & 1)] = acc[cb];
        else red[((tq * 5 + cb) * 7 + ((w - dst - 1) & 7)) * 64 + lane] = acc[cb];
      }
    }
    __syncthreads();
    STAMP(18);
    const LAS float* col = (const LAS float*)(L + L_COL);
    float v[12];
#pragma unroll
    for (int q = 0; q < 3; ++q) {
      const int cb = q == 2 ? 4 : 2 * kh + q;
      f32x4 s = keep[q];
#pragma unroll
      for (int i = 0; i < 7; ++i) s += red[((tb * 5 + cb) * 7 + i) * 64 + lane];
      const int n = q < 2 ? c0 + 4 * q : c1;
      const f32x4 gb = *(const LAS f32x4*)(col + 240 + n), b2 = *(const LAS f32x4*)(col + 320 + n);
#pragma unroll
      for (int e = 0; e < 4; ++e) {
        const unsigned xw = q < 2 ? x1a[2 * q + (e >> 1)] : x1b[e >> 1];
        const float xx = (e & 1) ? bfhi(xw) : bflo(xw);
        v[4 * q + e] = xx + gb[e] * (s[e] + b2[e]);
      }
    }
    uint16_t* op = p.out + (size_t)tok3 * D_ + j * 80;
    *(u32x4*)(op + c0) = u32x4{pk2(v[0], v[1]), pk2(v[2], v[3]), pk2(v[4], v[5]), pk2(v[6], v[7])};
    if (!kh) *(u32x2*)(op + c1) = u32x2{pk2(v[8], v[9]), pk2(v[10], v[11])};
    STAMP(19);
  }
}

extern "C" void kernel_launch(void* const* d_in, const int* in_sizes, int n_in, void* d_out, int out_size, void* d_ws, size_t ws_size, hipStream_t stream) {
  Params p;
  p.x = (const uint16_t*)d_in[0]; p.wq = (const uint16_t*)d_in[1]; p.wk = (const uint16_t*)d_in[2]; p.wv = (const uint16_t*)d_in[3]; p.wo = (const uint16_t*)d_in[4];
  p.w1 = (const uint16_t*)d_in[6]; p.b1 = (const uint16_t*)d_in[7]; p.w2 = (const uint16_t*)d_in[8]; p.b2 = (const uint16_t*)d_in[9];
  p.c = (const uint16_t*)d_in[10]; p.wmod = (const uint16_t*)d_in[11];
  p.out = (uint16_t*)d_out; p.ws = (unsigned char*)d_ws;
  (void)hipFuncSetAttribute((const void*)k_mega, hipFuncAttributeMaxDynamicSharedMemorySize, LDS_BYTES);
  k_prep<<<NB_MOD + NB_STAT + NB_RT, 256, 0, stream>>>(p);
  k_mega<<<256, 512, LDS_BYTES, stream>>>(p);
}
```

```cpp
#include <hip/hip_runtime.h>
#include <hip/hip_bf16.h>
#include <stdint.h>

typedef __attribute__((ext_vector_type(8))) __bf16 bf16x8;
typedef __attribute__((ext_vector_type(2))) __bf16 bf16x2;
typedef __attribute__((ext_vector_type(4))) short s16x4;
typedef __attribute__((ext_vector_type(16))) float f32x16;
typedef __attribute__((ext_vector_type(4))) float f32x4;
typedef __attribute__((ext_vector_type(2))) float f32x2;
typedef __attribute__((ext_vector_type(4))) unsigned u32x4;
typedef __attribute__((ext_vector_type(2))) unsigned u32x2;
#define DI __device__ __forceinline__
#define LAS __attribute__((address_space(3)))

constexpr int S_ = 2048, D_ = 640, F_ = 1728;

constexpr int WS_MOD   = 0;
constexpr int WS_EPO   = 16384;
constexpr int WS_FLG   = 32768;
constexpr int WS_KV    = 36864;
constexpr int WS_WOT   = 0x120000;
constexpr int WS_W1T   = 0x200000;
constexpr int WS_W2T   = 0x440000;
constexpr int WS_GRP   = 0x680000;
constexpr int G_O = 0, G_Z = 81920, G_ST = 163840, G_HH = 196608, GRP_SZ = 425984;
constexpr int WS_STAMP = WS_GRP + 32 * GRP_SZ;
constexpr int KVG = 2560;
constexpr int KVT = 2704;
constexpr int KV_K1 = 1024, KV_ONE = 1280, KV_V = 1296, KV_VS = 352;
constexpr unsigned SENT = 0xffffffffu;

struct Params {
  const uint16_t *x, *wq, *wk, *wv, *wo, *w1, *b1, *w2, *b2, *c, *wmod;
  uint16_t* out;
  unsigned char* ws;
};

DI float bf2f(uint16_t b) { return __builtin_bit_cast(float, (unsigned)b << 16); }
DI float bflo(unsigned u) { return __builtin_bit_cast(float, u << 16); }
DI float bfhi(unsigned u) { return __builtin_bit_cast(float, u & 0xffff0000u); }
DI unsigned pk2(float a, float b) { f32x2 v = {a, b}; return __builtin_bit_cast(unsigned, __builtin_convertvector(v, bf16x2)); }
DI float rbf(float a) { return bflo(pk2(a, 0.f)); }
DI f32x16 mfma32(u32x4 a, u32x4 b, f32x16 c) { return __builtin_amdgcn_mfma_f32_32x32x16_bf16(__builtin_bit_cast(bf16x8, a), __builtin_bit_cast(bf16x8, b), c, 0, 0, 0); }
DI f32x16 mfma32k8(u32x2 a, u32x2 b, f32x16 c) { return __builtin_amdgcn_mfma_f32_32x32x8bf16_1k(__builtin_bit_cast(s16x4, a), __builtin_bit_cast(s16x4, b), c, 0, 0, 0); }
DI f32x4 mfma16(u32x4 a, u32x4 b, f32x4 c) { return __builtin_amdgcn_mfma_f32_16x16x32_bf16(__builtin_bit_cast(bf16x8, a), __builtin_bit_cast(bf16x8, b), c, 0, 0, 0); }
DI float swap32(float v) {
  auto r = __builtin_amdgcn_permlane32_swap(__builtin_bit_cast(unsigned, v), __builtin_bit_cast(unsigned, v), false, false);
  return __builtin_bit_cast(float, (threadIdx.x & 32) ? r[0] : r[1]);
}

#define GAS __attribute__((address_space(1)))
#define SBAR() __builtin_amdgcn_sched_barrier(0)
#define DRAIN() asm volatile("s_waitcnt vmcnt(0)" ::: "memory")
#define CBAR() asm volatile("" ::: "memory")
#define ORDER() __builtin_amdgcn_fence(__ATOMIC_ACQUIRE, "wavefront")
typedef __amdgpu_buffer_rsrc_t rsrc_t;
DI u32x4 ld16s(rsrc_t rs, int off, int so = 0) { return __builtin_amdgcn_raw_buffer_load_b128(rs, off, so, 16); }
DI u32x2 ld8s(rsrc_t rs, int off, int so = 0) { return __builtin_amdgcn_raw_buffer_load_b64(rs, off, so, 16); }
#define WAUX 16
DI u32x4 ld16w(rsrc_t rs, int off, int so) { return __builtin_amdgcn_raw_buffer_load_b128(rs, off, so, WAUX); }
DI unsigned ld4s(rsrc_t rs, int off, int so = 0) { return __builtin_amdgcn_raw_buffer_load_b32(rs, off, so, 16); }
DI void st16s(rsrc_t rs, int off, int so, u32x4 v) { __builtin_amdgcn_raw_buffer_store_b128(v, rs, off, so, 16); }
DI void st8s(rsrc_t rs, int off, int so, u32x2 v) { __builtin_amdgcn_raw_buffer_store_b64(v, rs, off, so, 16); }
DI unsigned ldflag(const unsigned* p) { return __hip_atomic_load((const GAS unsigned*)p, __ATOMIC_RELAXED, __HIP_MEMORY_SCOPE_AGENT); }
DI void stflag(unsigned* p, unsigned v) { __hip_atomic_store((GAS unsigned*)p, v, __ATOMIC_RELAXED, __HIP_MEMORY_SCOPE_AGENT); }
constexpr int SPIN_MAX = 1 << 18;
DI void poll_more(unsigned v, const unsigned* p, unsigned tag) {
  if (__builtin_expect(!__all(v == tag), 0)) {
    int spins = 0;
_Pragma("unroll 1")
    do { __builtin_amdgcn_s_sleep(1); v = ldflag(p); } while (!__all(v == tag) && ++spins < SPIN_MAX);
  }
  ORDER();
}
DI unsigned cl(unsigned v) { return v < SENT - 1u ? v : SENT - 1u; }
DI unsigned pk2c(float a, float b) { return cl(pk2(a, b)); }
DI unsigned umax3(unsigned a, unsigned b, unsigned c) { return __builtin_elementwise_max(__builtin_elementwise_max(a, b), c); }
DI unsigned mx4(unsigned m, u32x4 v) { return umax3(umax3(m, v[0], v[1]), v[2], v[3]); }
DI bool pending(unsigned m) { return __any(m == SENT); }
#define STAMP(i) do { CBAR(); SBAR(); } while (0)
#define MARK(s_, i) SBAR()

constexpr int LDS_BYTES = 160 * 1024;
constexpr int L_WKV = 102400;
constexpr int L_VEC = 157184;
constexpr int L_WQ = 81920;
constexpr int L_MRD = 147456;
constexpr int L_RED = 133120;
constexpr int L_XCH = 98304;
constexpr int CHB = 4 * KVT;
constexpr int L_WO = 4 * CHB;
constexpr int L_OX2 = L_WO + 102400;
constexpr int L_R3 = 2 * CHB;
constexpr int L_Z = 0;
constexpr int L_GSC = 139264;
constexpr int L_SCAL = 159808;
constexpr int L_X1 = 144384;
constexpr int CHG = 4 * KVG;
constexpr int L_MUX = 156928;
constexpr int L_COL = 157184;
constexpr int L_B1 = 158784;
constexpr int L_V3 = 159808;
constexpr int L_CST = 163648;
constexpr int L_FLG = 163712;

template <class T> DI T gld(const void* base, unsigned off) { return *(const T*)((const char*)base + off); }
DI float dot2(unsigned a, unsigned b, float c) { return __builtin_amdgcn_fdot2_f32_bf16(__builtin_bit_cast(bf16x2, a), __builtin_bit_cast(bf16x2, b), c, false); }
DI float sum32(float a, float b) {
  auto r = __builtin_amdgcn_permlane32_swap(__builtin_bit_cast(unsigned, a), __builtin_bit_cast(unsigned, b), false, false);
  const unsigned r0 = r[0], r1 = r[1];
  return __builtin_bit_cast(float, r0) + __builtin_bit_cast(float, r1);
}
DI float sum16(float a, float b) {
  auto r = __builtin_amdgcn_permlane16_swap(__builtin_bit_cast(unsigned, a), __builtin_bit_cast(unsigned, b), false, false);
  const unsigned r0 = r[0], r1 = r[1];
  return __builtin_bit_cast(float, r0) + __builtin_bit_cast(float, r1);
}
template <int C> DI float dpp(float v) { return __builtin_bit_cast(float, __builtin_amdgcn_update_dpp(0, __builtin_bit_cast(int, v), C, 0xf, 0xf, true)); }
DI float row16_sum(float v) { v += dpp<0x128>(v); v += dpp<0x124>(v); v += dpp<0x122>(v); v += dpp<0x121>(v); return v; }
typedef __attribute__((ext_vector_type(4))) short v4i16_t;
DI u32x2 trd(const LAS unsigned char* a) { return __builtin_bit_cast(u32x2, __builtin_amdgcn_ds_read_tr16_b64_v4i16((LAS v4i16_t*)a)); }
DI void mod_put(LAS unsigned char* L, int pc, int j, u32x4 gv) {
  const int sec = pc / 160, q = pc - sec * 160;
  f32x4 v = {bflo(gv[0]), bfhi(gv[0]), bflo(gv[2]), bfhi(gv[2])};
  if (sec == 4) v = f32x4{rbf(1.f + v[0]), rbf(1.f + v[1]), rbf(1.f + v[2]), rbf(1.f + v[3])};
  {
    if (sec == 3 || sec == 4) *(LAS u32x2*)(L + L_V3 + (sec == 3 ? 1280 : 0) + q * 8) = u32x2{pk2(v[0], v[1]), pk2(v[2], v[3])};
    const unsigned ql = (unsigned)(q - j * 20);
    if (ql < 20u) *(LAS f32x4*)(L + L_COL + (sec == 2 ? 0 : sec == 4 ? 320 : sec == 3 ? 640 : 960) + ql * 16) = v;
  }
}
DI u32x4 panel_frag(LAS unsigned char* L, int tt, int ks, int lane) {
  const int c = lane & 31, h = lane >> 5;
  return *(const LAS u32x4*)(L + tt * 40960 + ks * 1024 + h * 512 + ((c ^ ((2 * ks + h) & 7)) * 16));
}

DI f32x16 attn_slow(rsrc_t rs, int lane, u32x4 q0, u32x2 q1) {
  const int r = lane & 31, h = lane >> 5;
  if (h) q1 = u32x2{0u, 0u};
  float m = -3.0e38f;
  f32x16 o = {};
  const int vrow = r > 19 ? 19 : r;
#pragma unroll 1
  for (int t = 0; t < 64; ++t) {
    const int tile = WS_KV + t * KVG;
    const u32x4 k0 = ld16s(rs, lane * 16, tile);
    const u32x2 k1 = ld8s(rs, KV_K1 + r * 8, tile);
    f32x16 s = mfma32(k0, q0, f32x16{});
    s = mfma32k8(k1, q1, s);
    float mx = s[0];
#pragma unroll
    for (int i = 1; i < 16; ++i) mx = fmaxf(mx, s[i]);
    mx = fmaxf(mx, swap32(mx));
    const float mn = fmaxf(m, mx), al = __builtin_amdgcn_exp2f(m - mn);
    m = mn;
#pragma unroll
    for (int i = 0; i < 16; ++i) { o[i] *= al; s[i] = __builtin_amdgcn_exp2f(s[i] - mn); }
#pragma unroll
    for (int sI = 0; sI < 2; ++sI) {
      u32x4 vf = ld16s(rs, h * 320 + vrow * 16, tile + 1280 + sI * 640);
      if (r >= 20) vf = u32x4{0x3f803f80u, 0x3f803f80u, 0x3f803f80u, 0x3f803f80u};
      const u32x4 pf = {pk2(s[8 * sI], s[8 * sI + 1]), pk2(s[8 * sI + 2], s[8 * sI + 3]), pk2(s[8 * sI + 4], s[8 * sI + 5]), pk2(s[8 * sI + 6], s[8 * sI + 7])};
      o = mfma32(vf, pf, o);
    }
  }
  return o;
}

template <int V> struct IC { static constexpr int v = V; };
template <int N, class F> DI void sfor(F&& f) { if constexpr (N > 0) { sfor<N - 1>(f); f(IC<N - 1>{}); } }
#define OPQ(x) asm volatile("" : "+v"(x))
#define STAGE(n, stmt) do { _Pragma("unroll") for (int e = 0; e < (n); ++e) { stmt; } SBAR(); } while (0)
DI void gelu16(const f32x16& a, float sc, unsigned (&o)[8]) {
  constexpr float C0 = -2.f * 0.7978845608028654f * 1.4426950408889634f;
  float x[16], t[16];
  SBAR();
  STAGE(16, x[e] = a[e] * sc; OPQ(x[e]));
  STAGE(16, t[e] = x[e] * x[e]; OPQ(t[e]));
  STAGE(16, t[e] = __builtin_fmaf(t[e], C0 * 0.044715f, C0); OPQ(t[e]));
  STAGE(16, t[e] *= x[e]; OPQ(t[e]));
  STAGE(16, t[e] = __builtin_amdgcn_exp2f(t[e]));
  STAGE(16, t[e] += 1.f; OPQ(t[e]));
  STAGE(16, t[e] = __builtin_amdgcn_rcpf(t[e]));
  STAGE(16, t[e] *= x[e]; OPQ(t[e]));
  STAGE(8, o[e] = pk2(t[2 * e], t[2 * e + 1]));
  STAGE(8, o[e] = cl(o[e]); OPQ(o[e]));
}
DI float tile_max(const f32x16& s) {
  float mx = s[0];
#pragma unroll
  for (int i = 1; i < 16; ++i) mx = fmaxf(mx, s[i]);
  return fmaxf(mx, swap32(mx));
}
DI float swap16(float v) {
  auto r = __builtin_amdgcn_permlane16_swap(__builtin_bit_cast(unsigned, v), __builtin_bit_cast(unsigned, v), false, false);
  return __builtin_bit_cast(float, (threadIdx.x & 16) ? r[0] : r[1]);
}

#define HK_WO 1
#define HK_FILL 1
#define HK_CHK 1
#define PFL2 1
#define NSE 2
#define NS2 4
#define NWA 4
#define PRA 0
#define PRB 0
#define PRB2 1
#define NOW2 0
#define PSLP 2
#define ASLP 12
#define LATEP 1
#define PFQ 2
#define RVP 0
#define PFM 1
#define TW 2
#define TWW 6
#define QCH 9
#define GC 2
#define RV0 10
#define RVA 0
#define RVB 0
#define PFR 0
#define XO 0
#define WQI 0
#define OKS 12
#define NPRE 31
#define REIDX(n) int tid##n = threadIdx.x; asm volatile("" : "+v"(tid##n)); const int tid = tid##n, lane = tid & 63, r = lane & 31, h = lane >> 5, t16 = tid * 16; (void)r; (void)h; (void)t16; (void)lane
#define RSLOT(b, i) ((b) * 7 + (i))
template <int N> DI void lds_wait(LAS unsigned char* f) {
_Pragma("unroll 1")
  for (int spins = 0; spins < SPIN_MAX; ++spins) {
    const u32x4 v = *(volatile LAS u32x4*)f;
    if (__builtin_amdgcn_readfirstlane(v[0] & v[1] & v[2] & v[3])) break;
    __builtin_amdgcn_s_sleep(1);
  }
  __builtin_amdgcn_fence(__ATOMIC_ACQUIRE, "workgroup");
}
#define RETRY(N, v, voff, soff, stride) RETRYF(N, v, voff, (soff) + i_ * (stride))
#define PROBE(voff, soff) do { u32x2 pr_; \
    _Pragma("unroll 1") do { __builtin_amdgcn_s_sleep(PSLP); CBAR(); pr_ = ld8s(rs, (voff) + 4, soff); } while (pending(umax3(pr_[0], pr_[1], 0u)) && ++spins_ < SPIN_MAX); } while (0)
#define RETRYF(N, v, voff, soff) do { \
    unsigned any_ = 0u; \
    _Pragma("unroll") for (int i_ = 0; i_ < N; ++i_) any_ = mx4(any_, v[i_]); \
    if (pending(any_)) { \
      int spins_ = 0; \
      _Pragma("unroll 1") do { \
        unsigned msk_ = 0u; \
        _Pragma("unroll") for (int i_ = 0; i_ < N; ++i_) msk_ |= pending(mx4(0u, v[i_])) ? 1u << i_ : 0u; \
        { const int i_ = 31 - __builtin_clz(msk_); PROBE(voff, soff); } \
        _Pragma("unroll") for (int i_ = 0; i_ < N; ++i_) if (msk_ & (1u << i_)) v[i_] = ld16s(rs, voff, soff); \
        any_ = 0u; \
        _Pragma("unroll") for (int i_ = 0; i_ < N; ++i_) any_ = mx4(any_, v[i_]); \
      } while (pending(any_) && ++spins_ < SPIN_MAX); \
    } } while (0)
#define EX(s, i) s[i] = __builtin_amdgcn_exp2f(s[i])
extern __shared__ __attribute__((aligned(16))) unsigned char smem[];

__global__ void __launch_bounds__(512, 2) k_mega(Params p) {
  LAS unsigned char* L = (LAS unsigned char*)smem;
  const int tid = threadIdx.x, lane = tid & 63, w = __builtin_amdgcn_readfirstlane(tid >> 6);
  const int xl = blockIdx.x & 7, rk = blockIdx.x >> 3, tg = xl * 4 + (rk >> 3), j = rk & 7;
  const int r = lane & 31, h = lane >> 5, cg = tid & 15, ri = tid >> 4;
  const rsrc_t rs = __builtin_amdgcn_make_buffer_rsrc((void*)p.ws, 0, 0x7fffffff, 0x00020000);
  unsigned* flg = (unsigned*)(p.ws + WS_FLG);
  const int grp = WS_GRP + tg * GRP_SZ;
  STAMP(0);
  u32x4 qa0, qb0; u32x2 qa1, qb1;
  unsigned tag;
  u32x2 rv[8]; bool rz = false; int rd, rds, rn;
  u32x4 st0; unsigned st1;
  int lp0, lp1;
  { const int t0 = tid / 160, q0 = tid % 160, q1 = 32 + (tid >> 2);
    lp0 = t0 * KVT + (q0 < 80 ? q0 * 16 : KV_V + ((q0 - 80) / 20) * KV_VS + ((q0 - 80) % 20) * 16);
    lp1 = 3 * KVT + (q1 < 80 ? q1 * 16 : KV_V + ((q1 - 80) / 20) * KV_VS + ((q1 - 80) % 20) * 16) + (tid & 3) * 4; }
  const int g1 = 8192 + tid * 4;
  const int t16 = tid * 16;
  {
    const int kind = j >> 1, tt = j & 1, hd = w & 3, th = w >> 2;
    const int mi = (rk >> 3) * 4 + (j & 3), lb = (mi >> 1) * 8 + xl, cbk = (lb < 60 ? lb : 59) * 2 + (mi & 1);
    const bool early = j >= 4 && lb < 20, late = j >= 4 && lb >= 20 && lb < 60;
    u32x4 aux[5]; uint16_t cv[5];
    auto modld = [&]() {
#pragma unroll
      for (int i = 0; i < 5; ++i) aux[i] = gld<u32x4>(p.wmod + cbk * 32 + i * 128 * 3840, (unsigned)(tid >> 2) * 7680u + (tid & 3) * 16);
#pragma unroll
      for (int i = 0; i < 5; ++i) cv[i] = gld<uint16_t>(p.c + i * 128, (unsigned)(tid >> 2) * 2u);
    };
    unsigned pm = 0u;
    if (PFM == 1 && tid < (xl < 4 ? 60 : 40)) pm = gld<unsigned>(p.wmod, (unsigned)((rk * 20 + tid % 20) * 7680 + (xl + 8 * (tid / 20)) * 128));
    SBAR();
    if (early) modld();
    else if (j < 4) {
      if (w == 0) { st16s(rs, t16, WS_KV + (tg * 2 + tt) * KVG + kind * 1280, u32x4{SENT, SENT, SENT, SENT}); st16s(rs, t16, WS_KV + (tg * 2 + tt) * KVG + kind * 1280 + 256, u32x4{SENT, SENT, SENT, SENT}); }
#pragma unroll
      for (int i = 0; i < 4; ++i) aux[i] = gld<u32x4>(kind ? p.wv : p.wk, (unsigned)(w * 3200 + i * 800 + (lane < 50 ? lane : 49) * 16));
    }
    const unsigned epo = ld4s(rs, 0, WS_EPO);
    SBAR();
    u32x4 xv[10];
#pragma unroll
    for (int n = 0; n < 10; ++n) {
      const int c = XO ? n % 5 : n >> 1, i = XO ? n / 5 : n & 1;
      xv[c * 2 + i] = gld<u32x4>(p.x + (tg * 64 + (i ^ tt) * 32) * D_, (unsigned)(ri * 1280 + cg * 16 + c * 256));
      SBAR();
    }
    SBAR();
    auto modcalc = [&]() {
      f32x2 a[4];
#pragma unroll
      for (int i = 0; i < 5; ++i) {
        const float c = bf2f(cv[i]), sc = rbf(c * __builtin_amdgcn_rcpf(1.f + __expf(-c)));
#pragma unroll
        for (int e = 0; e < 4; ++e) { const f32x2 t = f32x2{bflo(aux[i][e]), bfhi(aux[i][e])} * sc; a[e] = i ? a[e] + t : t; }
      }
#pragma unroll
      for (int e = 0; e < 8; ++e) { float v = a[e >> 1][e & 1]; v += dpp<0x128>(v); v += dpp<0x124>(v); a[e >> 1][e & 1] = v; }
      if ((lane & 12) == 0) { LAS f32x4* d = (LAS f32x4*)(L + L_MRD + (tid >> 4) * 128 + (lane & 3) * 32); d[0] = f32x4{a[0][0], a[0][1], a[1][0], a[1][1]}; d[1] = f32x4{a[2][0], a[2][1], a[3][0], a[3][1]}; }
    };
    auto modfin = [&]() {
      if (w == 0) {
        f32x2 s = {0.f, 0.f};
#pragma unroll
        for (int i = 0; i < 8; ++i) s += *(const LAS f32x2*)(L + L_MRD + i * 512 + lane * 8);
#pragma unroll
        for (int e = 0; e < 2; ++e) { s[e] = sum16(s[e], s[e]); s[e] = sum32(s[e], s[e]); }
        if (lane < 16) st8s(rs, lane * 8, WS_MOD + cbk * 128, u32x2{pk2(s[0], s[1]), (unsigned)__builtin_amdgcn_readfirstlane(epo) * 1664525u + 1013904223u});
      }
    };
    unsigned pq = 0u;
    if (PFQ == 3 && !early) asm volatile("" :: "v"(xv[0]));
    if ((PFQ == 1 || PFQ == 3) && lane < 25) pq = gld<unsigned>(p.wq, (unsigned)(rk * 25600 + w * 3200 + lane * 128));
    SBAR();
    if (early) { modcalc(); __syncthreads(); modfin(); }
    STAMP(11);
    SBAR();
    asm volatile("" :: "v"(xv[9]));
    tag = (unsigned)__builtin_amdgcn_readfirstlane(epo) * 1664525u + 1013904223u;
    if (j < 4 && w == 0) { DRAIN(); if (lane == 0) stflag(flg + tg * 4 + j, tag); }
    STAMP(1);
    SBAR();
    if (PFQ == 2 && lane < 25) pq = gld<unsigned>(p.wq, (unsigned)(rk * 25600 + w * 3200 + lane * 128));
    uint16_t b1v; u32x2 b2v;
    { const int rho = tid & 31, fl = rho < 23 ? rho : (rho > 27 ? 26 : rho - 1), ww = (tid >> 5) & 7; b1v = gld<uint16_t>(p.b1 + 216 * j, (unsigned)(4 * (27 * (ww & 1) + fl) + (ww >> 1)) * 2u);
      b2v = gld<u32x2>(p.b2 + j * 80, (unsigned)(tid < 20 ? tid : 0) * 8u); }
    SBAR();
    const int i16 = lane & 15, rowo = 8 * h + (i16 >> 2), colo = ((lane >> 4) & 1) * 32 + (i16 & 3) * 8;
    if (j < 4) {
#pragma unroll
      for (int i = 0; i < 4; ++i) *(LAS u32x4*)(L + w * 3328 + i * 800 + (lane < 50 ? lane : 49) * 16) = aux[i];
      __builtin_amdgcn_fence(__ATOMIC_RELEASE, "wavefront"); __builtin_amdgcn_wave_barrier(); ORDER();
      const LAS unsigned char* kp = L + w * 3328 + rowo * 40 + colo;
#pragma unroll
      for (int i = 0; i < 5; ++i) { const u32x2 a = trd(kp + i * 640), b = trd(kp + i * 640 + 160); aux[i] = u32x4{a[0], a[1], b[0], b[1]}; }
    }
    SBAR();
    u32x4 wr[14];
    const unsigned t5 = tid < 500 ? tid : 499, wro = ((t5 * 52429u) >> 19) * 1120u + t5 * 16u;
    float xn[2][40];
#pragma unroll
    for (int i = 0; i < 2; ++i) {
      float s = 0.f, q = 0.f;
#pragma unroll
      for (int c = 0; c < 5; ++c)
#pragma unroll
        for (int e = 0; e < 4; ++e) { s = dot2(xv[c * 2 + i][e], 0x3f803f80u, s); q = dot2(xv[c * 2 + i][e], xv[c * 2 + i][e], q); }
      const float mu = row16_sum(s) * (1.f / D_);
      const float rstd = __builtin_amdgcn_rsqf(fmaxf(row16_sum(q) * (1.f / D_) - mu * mu, 0.f) + 1e-5f), nb = -mu * rstd;
#pragma unroll
      for (int c = 0; c < 5; ++c)
#pragma unroll
        for (int e = 0; e < 4; ++e) {
          f32x2 t = f32x2{bflo(xv[c * 2 + i][e]), bfhi(xv[c * 2 + i][e])} * rstd + nb;
          asm volatile("" : "+v"(t));
          xn[i][c * 8 + 2 * e] = t[0]; xn[i][c * 8 + 2 * e + 1] = t[1];
        }
      if (cg == 0) *(LAS float*)(L + L_MUX + (ri + 32 * (i ^ tt)) * 4) = mu;
      SBAR();
      if (i == 0) {
#pragma unroll
        for (int n = 0; n < 7 * WQI; ++n) wr[n] = gld<u32x4>(p.wq + j * 80 + ((n / 7) * 320 + (n % 7 < 6 ? (n % 7) * 50 : 270)) * 640, wro);
      }
      SBAR();
    }
    SBAR();
    if (tid < 160) *(LAS u32x2*)(L + L_V3 + 2560 + tid * 8) = u32x2{0x3f803f80u, 0x3f803f80u};
    if (tid < 12) *(LAS unsigned*)(L + L_FLG + tid * 4) = 0u;
    const uint16_t* rsrc_ = w < 3 ? p.w1 + 216 * j : (w < 6 ? p.w2 : p.wo) + j * 80; unsigned ro, rld, rstp, pfo;
    {
      if (w < 3) {
        const bool ract = tid < 135; const int item = tg * 135 + (ract ? tid : 0), ko = item / 54, fq = item % 54;
        ro = ko * 8 * F_ + 4 * fq; rld = F_; rstp = 4 * F_; pfo = ro + (lane & 7) * F_;
        rd = WS_W1T + (j * 80 + ko) * 3456 + fq * 16; rds = 864; rn = ract ? 4096 : 0;
      } else {
        const bool isw2 = w < 6;
        const bool ract = isw2 ? tid - 192 < 140 : (w == 6 && lane < 50);
        const int item = isw2 ? tg * 140 + (ract ? tid - 192 : 0) : tg * 50 + (ract ? lane : 0);
        const int ks = item / 80, i80 = item % 80, cb = i80 >> 4, pcl = (i80 >> 2) & 3, gq = i80 & 3;
        const int nl = cb < 4 ? 32 * (cb >> 1) + 8 * gq + 4 * (cb & 1) : 64 + 4 * gq;
        if (isw2) {
          const int sl = ks / 7, p28 = (ks % 7) * 4 + pcl, jl = p28 >= 14, pc = p28 - 14 * jl;
          int vv, l0;
          if (pc < 12) { const int a = pc % 3; vv = pc / 3; l0 = a == 2 ? 16 : 4 * a; rstp = (a == 2 ? 7 : 8) * 4 * 640; } else { vv = 2 * (pc - 12); l0 = 20; rstp = 27 * 4 * 640; rz = true; }
          vv += sl & 4;
          ro = (216 * (2 * (sl & 3) + jl) + 4 * (27 * (vv & 1) + l0) + (vv >> 1)) * 640 + nl; rld = 4 * 640;
          rd = WS_W2T + j * 286720;
        } else {
          const int pc = ks * 4 + pcl, jw = pc / 10, q = pc % 10;
          const int kb = q < 8 ? (4 * jw + (q >> 1)) * 20 + 4 * (q & 1) : (4 * jw + 2 * (q - 8)) * 20 + 16; rstp = (q < 8 ? 8 : 20) * 640; rld = 640;
          ro = kb * 640 + nl;
          rd = WS_WOT + j * 102400;
        }
        pfo = ro - nl + (cb < 2 ? 0 : cb < 4 ? 78 : 40) + gq * rld + (cb & 1) * rstp;
        rd += (isw2 ? tg * 140 + (w - 3) * 64 : tg * 50) * 64 + lane * 16; rds = 1024; rn = (isw2 ? (w == 5 ? 12 : 64) : (w == 6 ? 50 : 0)) * 64 - lane * 16;
      }
    }
    asm volatile("" : "+v"(ro), "+v"(rd), "+v"(rn), "+v"(pfo));
    STAMP(2);
#pragma unroll
    for (int n = 7 * WQI; n < 14; ++n) wr[n] = gld<u32x4>(p.wq + j * 80 + ((n / 7) * 320 + (n % 7 < 6 ? (n % 7) * 50 : 270)) * 640, wro);
    const int gvo = w == 2 ? (lane & 31) * 16 : lane * 16, gso = w == 2 ? WS_FLG : WS_MOD + (w - 3) * 1024;
    u32x4 gm;
    if (w >= 2) gm = ld16s(rs, gvo, gso);
    SBAR();
    LAS unsigned char* wp = L + L_WQ + (tid < 500 ? t16 : 7984);
    const LAS unsigned char* qp = L + L_WQ + (th * 160 + rowo) * 160 + hd * 40 + colo;
#pragma unroll
    for (int i = 0; i < 7; ++i) *(LAS u32x4*)(wp + (i < 6 ? i * 8000 : 43200)) = wr[i];
    u32x4 wq[20];
    asm volatile("" :: "v"(wr[13]));
    STAMP(9);
    SBAR();
    if (w >= 2) {
      const unsigned am = w == 2 ? ~0u : 0u;
_Pragma("unroll 1")
      for (int spins = 0; __any(((gm[1] ^ tag) | (gm[3] ^ tag) | (((gm[0] ^ tag) | (gm[2] ^ tag)) & am)) != 0u) && spins < SPIN_MAX; ++spins) { CBAR(); gm = ld16s(rs, gvo, gso); }
      const int pc = tid - 192, q = pc < 160 ? pc : pc - 160;
      f32x4 v = {bflo(gm[0]), bfhi(gm[0]), bflo(gm[2]), bfhi(gm[2])};
      const unsigned ga = pk2(1.f + v[0], 1.f + v[1]), gb = pk2(1.f + v[2], 1.f + v[3]);
      if (pc >= 160) v = f32x4{bflo(ga), bfhi(ga), bflo(gb), bfhi(gb)};
      if (w >= 3) *(LAS f32x4*)(L + L_VEC + (pc < 160 ? 2560 : 0) + (q & 1) * 1280 + (q >> 1) * 16) = v;
    }
    __builtin_amdgcn_s_waitcnt(0x0F70);
    __syncthreads();
    STAMP(3);
    asm volatile("" :: "v"(pm), "v"(pq));
    if (PFR && w < 7) pq = gld<uint16_t>(rsrc_, pfo * 2u);
    if (j >= 4 && tid < 200) pm = gld<unsigned>(p.wmod, (unsigned)((mi * 40 + tid % 40) * 7680 + (xl + 8 * (tid / 40 + (xl < 4 ? 3 : 2))) * 128));
    SBAR();
    f32x4 gs[5][4];
#pragma unroll
    for (int c = 0; c < 5; ++c)
#pragma unroll
      for (int q = 0; q < 4; ++q) gs[c][q] = *(const LAS f32x4*)(L + L_VEC + cg * 16 + q * 1280 + c * 256);
    SBAR();
    auto panel = [&](const int i) __attribute__((always_inline)) {
#pragma unroll
      for (int c = 0; c < 5; ++c) {
        const f32x4 ga = gs[c][0], gb = gs[c][1], s0 = gs[c][2], s1 = gs[c][3];
        const int kp = cg + 16 * c;
        const float* y = &xn[i][c * 8];
        const u32x4 o = {pk2(y[0] * ga[0] + s0[0], y[1] * ga[1] + s0[1]), pk2(y[2] * ga[2] + s0[2], y[3] * ga[3] + s0[3]), pk2(y[4] * gb[0] + s1[0], y[5] * gb[1] + s1[1]), pk2(y[6] * gb[2] + s1[2], y[7] * gb[3] + s1[3])};
        *(LAS u32x4*)(L + (i ^ tt) * 40960 + (kp >> 1) * 1024 + (kp & 1) * 512 + ((ri ^ (kp & 7)) * 16)) = o;
      }
    };
#pragma unroll
    for (int q = 0; q < RVA; ++q) rv[q] = gld<u32x2>(rsrc_, (ro + (q & 3) * rld + (q >> 2) * rstp) * 2u);
    SBAR();
    panel(0);
    SBAR();
    panel(1);
    SBAR();
#pragma unroll
    for (int q = RVA; q < RVB; ++q) rv[q] = gld<u32x2>(rsrc_, (ro + (q & 3) * rld + (q >> 2) * rstp) * 2u);
    SBAR();
    if (j < 4) __syncthreads();
    STAMP(4);
      LAS unsigned char* red = L + L_RED + (kind ? h * 320 + r * 16 : lane * 16);
      const int qst = kind ? 640 : 1024;
    if (j < 4) {
      f32x16 acc = {};
      u32x4 hf[5];
#pragma unroll
      for (int i = 0; i < 5; ++i) hf[i] = panel_frag(L, tt, w * 5 + i, lane);
      SBAR();
      if (kind == 0) {
#pragma unroll
        for (int i = 0; i < 5; ++i) acc = mfma32(aux[i], hf[i], acc);
      } else {
#pragma unroll
        for (int i = 0; i < 5; ++i) acc = mfma32(hf[i], aux[i], acc);
      }
#pragma unroll
      for (int q = 0; q < 4; ++q) if (kind ? r < 20 : (q < 2 || (q == 2 && !h))) *(LAS f32x4*)(red + w * 2560 + q * qst) = f32x4{acc[4 * q], acc[4 * q + 1], acc[4 * q + 2], acc[4 * q + 3]};
    }
    SBAR();
#pragma unroll
    for (int i = 0; i < 10; ++i) { const u32x2 a = trd(qp + i * 2560), b = trd(qp + i * 2560 + 640); wq[i] = u32x4{a[0], a[1], b[0], b[1]}; }
    __syncthreads();
    STAMP(10);
    if (j < 4) {
      if (w < 4) {
        f32x4 t[8];
#pragma unroll
        for (int ww = 0; ww < 8; ++ww) t[ww] = *(const LAS f32x4*)(red + w * qst + ww * 2560);
        SBAR();
        const f32x4 s = ((t[0] + t[1]) + (t[2] + t[3])) + ((t[4] + t[5]) + (t[6] + t[7]));
        u32x2 o = {pk2c(s[0], s[1]), pk2c(s[2], s[3])};
        const int tile = WS_KV + (tg * 2 + tt) * KVG;
        if (kind == 0) {
          if (w < 2) st8s(rs, lane * 16, tile + w * 8, o);
          else if (w == 2 && !h) st8s(rs, lane * 8, tile + KV_K1, o);
        } else if (r < 20) st8s(rs, h * 320 + r * 16, tile + 1280 + (w >> 1) * 640 + (w & 1) * 8, o);
      }
    }
    STAMP(8);
#pragma unroll
    for (int i = 0; i < 7; ++i) *(LAS u32x4*)(wp + (i < 6 ? i * 8000 : 43200)) = wr[7 + i];
    __syncthreads();
    STAMP(12);
    SBAR();
    SBAR();
    SBAR();
    if (tid < 256) *(LAS float*)(L + L_B1 + tid * 4) = bf2f(b1v);
    if (tid < 20) *(LAS f32x4*)(L + L_COL + 1280 + tid * 16) = f32x4{bflo(b2v[0]), bfhi(b2v[0]), bflo(b2v[1]), bfhi(b2v[1])};
    u32x4 s0, s1; unsigned s2, s3;
    const int c0o = WS_KV + (2 * xl) * CHG;
    f32x16 a0 = {}, a1 = {};
    {
      u32x4 pf[3][2];
#pragma unroll
      for (int i = 0; i < 3; ++i) { pf[i][0] = panel_frag(L, 0, th * 10 + i, lane); pf[i][1] = panel_frag(L, 1, th * 10 + i, lane); }
      SBAR();
#pragma unroll
      for (int i = 0; i < 20; ++i) {
        const int kn = th * 10 + ((i + 3) / 10) * 20 + (i + 3) % 10;
        a0 = mfma32(wq[i], pf[i % 3][0], a0);
        if (i + 3 < 20) pf[i % 3][0] = panel_frag(L, 0, kn, lane);
        u32x2 ta, tb;
        if (i < 10) ta = trd(qp + i * 2560);
        SBAR();
        a1 = mfma32(wq[i], pf[i % 3][1], a1);
        if (i + 3 < 20) pf[i % 3][1] = panel_frag(L, 1, kn, lane);
        if (i < 10) { tb = trd(qp + i * 2560 + 640); wq[10 + i] = u32x4{ta[0], ta[1], tb[0], tb[1]}; }
        SBAR();
        if (i >= RV0 && i < RV0 + 8 - RVB) { const int q = (i - RV0 + RVB) & 7; rv[q] = gld<u32x2>(rsrc_, (ro + (q & 3) * rld + (q >> 2) * rstp) * 2u); }
        if (late && i < 5) {
          aux[i] = gld<u32x4>(p.wmod + cbk * 32 + i * 128 * 3840, (unsigned)(tid >> 2) * 7680u + (tid & 3) * 16);
          cv[i] = gld<uint16_t>(p.c + i * 128, (unsigned)(tid >> 2) * 2u);
        }
        SBAR();
        if (i == QCH) {
    CBAR();
    s0 = ld16s(rs, t16, c0o); s1 = ld16s(rs, t16, c0o + CHG);
    s2 = ld4s(rs, g1, c0o); s3 = ld4s(rs, g1, c0o + CHG);
    { const int c = WS_KV + ((2 * xl + 2) & 15) * CHG; st0 = ld16s(rs, t16, c); st1 = ld4s(rs, g1, c); }
    SBAR();
        }
      }
      OPQ(a0); OPQ(a1);
    }
    SBAR();
    STAMP(5);
    SBAR();
    if (late) modcalc();
    __syncthreads();
    if (late) modfin();
    if (tid < 16 * 9) {
      const int t = tid / 9, q = tid % 9;
      const unsigned c = (q & 1) ? 0x3f803f80u : 0u;
      *(LAS u32x4*)(L + t * KVT + (q == 0 ? KV_ONE : KV_V + ((q - 1) >> 1) * KV_VS + 320 + ((q - 1) & 1) * 16)) = u32x4{q == 0 ? 0x3f80u : c, c, c, c};
    }
    LAS f32x4* xch = (LAS f32x4*)(L + L_XCH);
#pragma unroll
    for (int q = 0; q < 3; ++q) {
      xch[(w * 6 + q) * 64 + lane] = f32x4{a0[4 * q], a0[4 * q + 1], a0[4 * q + 2], a0[4 * q + 3]};
      xch[(w * 6 + 3 + q) * 64 + lane] = f32x4{a1[4 * q], a1[4 * q + 1], a1[4 * q + 2], a1[4 * q + 3]};
    }
    if (__builtin_expect(pending(mx4(mx4(umax3(s2, s3, 0u), s0), s1)), 0)) {
      int spins = 0;
_Pragma("unroll 1")
      do {
        CBAR();
        s0 = ld16s(rs, t16, c0o); s1 = ld16s(rs, t16, c0o + CHG);
        s2 = ld4s(rs, g1, c0o); s3 = ld4s(rs, g1, c0o + CHG);
      } while (pending(mx4(mx4(umax3(s2, s3, 0u), s0), s1)) && ++spins < SPIN_MAX);
    }
    asm volatile("" :: "v"(pm), "v"(pq));
    *(LAS u32x4*)(L + lp0) = s0; *(LAS u32x4*)(L + CHB + lp0) = s1;
    *(LAS unsigned*)(L + lp1) = s2; *(LAS unsigned*)(L + CHB + lp1) = s3;
    SBAR();
#pragma unroll
    for (int q = 0; q < 8; ++q) if (RV0 > 20) rv[q] = gld<u32x2>(rsrc_, (ro + (q & 3) * rld + (q >> 2) * rstp) * 2u);
    __syncthreads();
    const float qs = 0.22360679774997896f * 1.4426950408889634f;
#pragma unroll
    for (int q = 0; q < 3; ++q) {
      const f32x4 oa = xch[((w ^ 4) * 6 + q) * 64 + lane], ob = xch[((w ^ 4) * 6 + 3 + q) * 64 + lane];
#pragma unroll
      for (int e = 0; e < 4; ++e) { a0[4 * q + e] = (a0[4 * q + e] + oa[e]) * qs; a1[4 * q + e] = (a1[4 * q + e] + ob[e]) * qs; }
    }
    qa0 = u32x4{pk2(a0[0], a0[1]), pk2(a0[2], a0[3]), pk2(a0[4], a0[5]), pk2(a0[6], a0[7])};
    qb0 = u32x4{pk2(a1[0], a1[1]), pk2(a1[2], a1[3]), pk2(a1[4], a1[5]), pk2(a1[6], a1[7])};
    qa1 = u32x2{pk2(a0[8], a0[9]), pk2(a0[10], a0[11])};
    qb1 = u32x2{pk2(a1[8], a1[9]), pk2(a1[10], a1[11])};
    if (h) { qa1 = u32x2{0u, 0u}; qb1 = u32x2{0u, 0u}; }
    STAMP(6);
  }
  u32x4 w1f[40];
  const int w1o = WS_W1T + j * 276480 + w * 432, w1v = h * 3456 + (r < 23 ? r : (r > 27 ? 26 : r - 1)) * 16;
  const int kh = w >> 2;
  {
    const LAS unsigned char* pk0 = L + lane * 16;
    const LAS unsigned char* pk1 = L + (h ? KV_ONE : KV_K1 + r * 8);
    const LAS unsigned char* pv = L + KV_V + h * KV_VS + (r > 21 ? 21 : r) * 16 + kh * 2 * KVT;
    {
      const u32x4 k0 = *(const LAS u32x4*)pk0; const u32x2 k1 = *(const LAS u32x2*)pk1;
      const float ma = tile_max(mfma32k8(k1, qa1, mfma32(k0, qa0, f32x16{})));
      const float mb = tile_max(mfma32k8(k1, qb1, mfma32(k0, qb0, f32x16{})));
      if (h) { qa1[0] = pk2(-ma, 0.f); qb1[0] = pk2(-mb, 0.f); }
    }
    pk0 += kh * 2 * KVT; pk1 += kh * 2 * KVT;
    const int woo = WS_WOT + j * 102400;
    const int fo = grp + j * 53248;
    unsigned fv = 0u, mxa = 0u;
    u32x4 wst, g0, g1_;
    const int m1 = (tid & 127) * 16;
    u32x4 k0 = *(const LAS u32x4*)pk0; u32x2 k1 = *(const LAS u32x2*)pk1;
    u32x4 v0 = *(const LAS u32x4*)pv, v1, p0, p1;
    f32x16 sa = mfma32k8(k1, qa1, mfma32(k0, qa0, f32x16{})), sb, oa = {}, ob = {};
    sfor<32>([&](auto CU) __attribute__((always_inline)) {
      {
        constexpr int c = decltype(CU)::v >> 1, u = decltype(CU)::v & 1;
        const int off = (c & 3) * CHB + u * KVT, on = u ? ((c + 1) & 3) * CHB : off + KVT;
        const bool first = c == 0 && u == 0, last = c == 15 && u == 1, bar = u == 1 && (c & 1) && c < 15;
        const int nsrc = WS_KV + ((2 * xl + c + 3) & 15) * CHG;
        SBAR(); EX(sa, 0); EX(sa, 1); SBAR();
        if (!first) ob = mfma32(v1, p1, ob);
        SBAR();
        v1 = *(const LAS u32x4*)(pv + off + 2 * KV_VS); SBAR();
        EX(sa, 2); EX(sa, 3); EX(sa, 4); EX(sa, 5); SBAR();
        sb = mfma32(k0, qb0, f32x16{}); SBAR();
        if (!last && !bar) k0 = *(const LAS u32x4*)(pk0 + on);
        SBAR();
        EX(sa, 6); EX(sa, 7); p0[0] = pk2(sa[0], sa[1]); p0[1] = pk2(sa[2], sa[3]); p0[2] = pk2(sa[4], sa[5]); p0[3] = pk2(sa[6], sa[7]); EX(sa, 8); SBAR();
        sb = mfma32k8(k1, qb1, sb); SBAR();
        if (!last && !bar) k1 = *(const LAS u32x2*)(pk1 + on);
        if (u == 1 && c == 14) fv = ldflag(flg + 256 + tg * 8 + (lane & 7));
        if (u == 0 && c == GC) { g0 = ld16s(rs, t16, WS_MOD + 5120); g1_ = ld16s(rs, m1, WS_MOD + 13312); }
        if (u == 0 && c == 3) fv = ldflag(flg + 512 + j * 32 + r);
        SBAR();
        EX(sa, 9); EX(sa, 10); EX(sa, 11); EX(sa, 12); SBAR();
        oa = mfma32(v0, p0, oa); SBAR();
        if (u == 0) {
          if (c <= 13) {
            mxa = mx4(umax3(mxa, st1, 0u), st0);
            *(LAS u32x4*)(L + ((c + 2) & 3) * CHB + lp0) = st0;
            *(LAS unsigned*)(L + ((c + 2) & 3) * CHB + lp1) = st1;
          } else if (c == 14 && w < 4) *(LAS u32x4*)(L + L_WO + 12 * 8192 + t16) = st0;
        } else {
          if (bar) {
            asm volatile("" :: "v"(sa[8]), "v"(sa[9]), "v"(sa[10]), "v"(sa[11]), "v"(sa[12]));
            if (__builtin_expect(pending(mxa), 0)) {
_Pragma("unroll 1")
              for (int cc = c + 1; cc <= c + 2; ++cc) {
                const int src = WS_KV + ((2 * xl + cc) & 15) * CHG;
                u32x4 a; unsigned b; int spins = 0;
_Pragma("unroll 1")
                do { CBAR(); a = ld16s(rs, t16, src); b = ld4s(rs, g1, src); } while (pending(mx4(b, a)) && ++spins < SPIN_MAX);
                *(LAS u32x4*)(L + (cc & 3) * CHB + lp0) = a;
                *(LAS unsigned*)(L + (cc & 3) * CHB + lp1) = b;
              }
              mxa = 0u;
            }
            if (c == 3) {
    if (__builtin_expect(!__all(g0[1] == tag && g0[3] == tag && g1_[1] == tag && g1_[3] == tag), 0)) {
      int spins = 0;
_Pragma("unroll 1")
      do { CBAR(); g0 = ld16s(rs, t16, WS_MOD + 5120); g1_ = ld16s(rs, m1, WS_MOD + 13312); } while (!__all(g0[1] == tag && g0[3] == tag && g1_[1] == tag && g1_[3] == tag) && ++spins < SPIN_MAX);
    }
    mod_put(L, tid + 320, j, g0);
    if (tid < 128) mod_put(L, tid + 832, j, g1_);
            }
            if (c == 5) {
    if (w == 7) {
      float cs[5] = {0.f, 0.f, 0.f, 0.f, 0.f};
#pragma unroll
      for (int i = 0; i < 5; ++i) {
        const unsigned gw = *(const LAS unsigned*)(L + L_V3 + (lane + 64 * i) * 4), sw = *(const LAS unsigned*)(L + L_V3 + 1280 + (lane + 64 * i) * 4);
        cs[0] = dot2(gw, 0x3f803f80u, cs[0]); cs[1] = dot2(gw, gw, cs[1]); cs[2] = dot2(gw, sw, cs[2]); cs[3] = dot2(sw, 0x3f803f80u, cs[3]); cs[4] = dot2(sw, sw, cs[4]);
      }
#pragma unroll
      for (int i = 0; i < 5; ++i) { cs[i] = row16_sum(cs[i]); cs[i] = sum16(cs[i], cs[i]); cs[i] = sum32(cs[i], cs[i]); }
      if (lane == 0) { *(LAS f32x4*)(L + L_CST) = f32x4{cs[0], cs[1], cs[2], cs[3]}; *(LAS float*)(L + L_CST + 16) = cs[4]; }
    }
            }
            if (c == 3) poll_more(fv, flg + 512 + j * 32 + r, tag);
            if (c == 1) asm volatile("s_waitcnt vmcnt(2)" ::: "memory");
            if (c == 7) asm volatile("s_waitcnt vmcnt(4)" ::: "memory");
            __syncthreads();
            k0 = *(const LAS u32x4*)(pk0 + on); k1 = *(const LAS u32x2*)(pk1 + on);
            if (c == 1 && tid == 0) stflag(flg + 512 + j * 32 + tg, tag);
            if (c == 7 && tid == 0) stflag(flg + 256 + tg * 8 + j, tag);
          }
          if (c >= 3 && c < 15) wst = ld16w(rs, t16, woo + (c - 3) * 8192);
          if (c >= 1 && c <= 3) st16s(rs, t16, fo + (2 * c - 2) * 8192, u32x4{SENT, SENT, SENT, SENT});
          if (c == 0) {
    {
      u32x4 rt[4];
#pragma unroll
      for (int e = 0; e < 4; ++e)
#pragma unroll
        for (int i = 0; i < 4; ++i) {
          unsigned hi = rv[2 * i + 1][e >> 1];
          if (i & 1) hi = rz ? 0u : hi;
          rt[e][i] = __builtin_amdgcn_perm(hi, rv[2 * i][e >> 1], (e & 1) ? 0x07060302u : 0x05040100u);
        }
      if (w >= 3 && w < 7) {
        LAS unsigned char* bp = L + L_WO + (w - 3) * 4096;
#pragma unroll
        for (int e = 0; e < 4; ++e) *(LAS u32x4*)(bp + lane * 64 + e * 16) = rt[e];
        __builtin_amdgcn_fence(__ATOMIC_RELEASE, "wavefront"); __builtin_amdgcn_wave_barrier(); ORDER();
#pragma unroll
        for (int e = 0; e < 4; ++e) rt[e] = *(const LAS u32x4*)(bp + e * 1024 + lane * 16);
      }
#pragma unroll
      for (int e = 0; e < 4; ++e) st16s(rs, rn > e * 1024 ? rd : (int)0x80000000, e * rds, rt[e]);
    }
          }
        }
        SBAR();
        EX(sa, 13); EX(sa, 14); EX(sa, 15); p1[0] = pk2(sa[8], sa[9]); p1[1] = pk2(sa[10], sa[11]); p1[2] = pk2(sa[12], sa[13]); p1[3] = pk2(sa[14], sa[15]); SBAR();
        EX(sb, 0); EX(sb, 1); SBAR();
        oa = mfma32(v1, p1, oa); SBAR();
        if (u == 0) { if (c <= 12) st0 = ld16s(rs, t16, nsrc); else if (c == 13 && w < 4) st0 = ld16w(rs, t16, woo + 12 * 8192); }
        else if (c >= 3 && 3 * (c - 3) < NPRE) w1f[c < 3 ? 0 : 3 * (c - 3)] = ld16w(rs, w1v, w1o + (3 * (c - 3)) * 6912);
        SBAR();
        EX(sb, 2); EX(sb, 3); EX(sb, 4); EX(sb, 5); SBAR();
        if (!last) sa = mfma32(k0, qa0, f32x16{});
        SBAR();
        if (u == 0) { if (c <= 12) st1 = ld4s(rs, g1, nsrc); }
        if (PFL2 && u == 0 && c >= 13) {
          asm volatile("" :: "v"(st1));
          if (c < 15) st1 = ld4s(rs, (lane < 35 ? lane : 34) * 128, WS_W2T + j * 286720 + ((rk >> 3) * 560 + w * 70 + (c - 13) * 35) * 128);
          else st1 = ld4s(rs, (lane < 16 ? lane : 15) * 128, WS_W1T + j * 276480 + NPRE * 6912 + ((rk >> 3) * 8 + w) * ((40 - NPRE) * 216));
        }
        if (u == 1 && c >= 3 && 3 * (c - 3) + 1 < NPRE) w1f[c < 3 ? 0 : 3 * (c - 3) + 1] = ld16w(rs, w1v, w1o + (3 * (c - 3) + 1) * 6912);
        SBAR();
        EX(sb, 6); EX(sb, 7); p0[0] = pk2(sb[0], sb[1]); p0[1] = pk2(sb[2], sb[3]); p0[2] = pk2(sb[4], sb[5]); p0[3] = pk2(sb[6], sb[7]); EX(sb, 8); SBAR();
        if (!last) sa = mfma32k8(k1, qa1, sa);
        SBAR();
        if (u == 0) { if (HK_WO && c >= 4) *(LAS u32x4*)(L + L_WO + (c - 4) * 8192 + t16) = wst; if (c == 4 && w < 4) st16s(rs, t16, fo + 6 * 8192, u32x4{SENT, SENT, SENT, SENT}); }
        else if (c >= 1 && c <= 3) st16s(rs, t16, fo + (2 * c - 1) * 8192, u32x4{SENT, SENT, SENT, SENT});
        if (u == 1 && c >= 3 && 3 * (c - 3) + 2 < NPRE) w1f[c < 3 ? 0 : 3 * (c - 3) + 2] = ld16w(rs, w1v, w1o + (3 * (c - 3) + 2) * 6912);
        SBAR();
        EX(sb, 9); EX(sb, 10); EX(sb, 11); EX(sb, 12); SBAR();
        ob = mfma32(v0, p0, ob); SBAR();
        if (!last) v0 = *(const LAS u32x4*)(pv + on);
        SBAR();
        EX(sb, 13); EX(sb, 14); EX(sb, 15); p1[0] = pk2(sb[8], sb[9]); p1[1] = pk2(sb[10], sb[11]); p1[2] = pk2(sb[12], sb[13]); p1[3] = pk2(sb[14], sb[15]); SBAR();
      }
    });
    ob = mfma32(v1, p1, ob);
    STAMP(7);
    MARK(2, 0);
    {
      const f32x16 snd = kh ? oa : ob;
#pragma unroll
      for (int q = 0; q < 3; ++q) *(LAS f32x4*)(L + (q < 2 ? (w * 2 + q) * 1024 : L_OX2 + w * 1024) + lane * 16) = f32x4{snd[4 * q], snd[4 * q + 1], snd[4 * q + 2], snd[4 * q + 3]};
    }
    f32x16 o = kh ? ob : oa;
    poll_more(fv, flg + 256 + tg * 8 + (lane & 7), tag);
    MARK(2, 1);
    __syncthreads();
    MARK(2, 2);
#pragma unroll
    for (int q = 0; q < 3; ++q) {
      const f32x4 t = *(const LAS f32x4*)(L + (q < 2 ? ((w ^ 4) * 2 + q) * 1024 : L_OX2 + (w ^ 4) * 1024) + lane * 16);
#pragma unroll
      for (int e = 0; e < 4; ++e) o[4 * q + e] += t[e];
    }
    if (__any(h && !(o[8] < 1.0e12f))) o = attn_slow(rs, lane, kh ? qb0 : qa0, kh ? qb1 : qa1);
    const float o8 = o[8]; const unsigned lu = __builtin_amdgcn_permlane32_swap(__builtin_bit_cast(unsigned, o8), __builtin_bit_cast(unsigned, o8), false, false)[1];
    const float rl = __builtin_amdgcn_rcpf(__builtin_bit_cast(float, lu));
    const int hd = w & 3, tok = kh * 32 + r;
    const int op = (tok >> 4) * 20480 + (tok & 15) * 16;
    float on[12]; unsigned ou[6];
    STAGE(12, on[e] = o[e] * rl; OPQ(on[e]));
    STAGE(6, ou[e] = pk2(on[2 * e], on[2 * e + 1]));
    STAGE(6, ou[e] = cl(ou[e]); OPQ(ou[e]));
    st16s(rs, op + h * 256, grp + G_O + (j * 10 + hd * 2) * 256, u32x4{ou[0], ou[1], ou[2], ou[3]});
    if (!h) st8s(rs, op, grp + G_O + (j * 10 + 8 + (hd >> 1)) * 256 + (hd & 1) * 8, u32x2{ou[4], ou[5]});
    MARK(2, 3);
    __builtin_amdgcn_s_barrier();
  }
  const int tb = w & 3;
  f32x16 cc = {};
  u32x4 w2f[35];
  {
    REIDX(3);
    const int tk = lane & 15, g = lane >> 4, w1v = h * 3456 + (r < 23 ? r : (r > 27 ? 26 : r - 1)) * 16;
    const int tok3 = tg * 64 + tb * 16 + tk, c0 = kh * 32 + g * 8, c1 = 64 + g * 4;
    const LAS unsigned char* vp = L + L_V3 + (r < 2 ? r : 2) * 1280 + h * 16;
    const u32x4 xa = gld<u32x4>(p.x + j * 80, (unsigned)(tok3 * D_ + c0) * 2u);
    const u32x2 xb = gld<u32x2>(p.x + j * 80, (unsigned)(tok3 * D_ + c1) * 2u);
    const int op = grp + G_O + tb * 20480 + kh * 10240;
#pragma unroll
    for (int i = NPRE; i < 32; ++i) w1f[i] = ld16w(rs, w1v, w1o + i * 6912);
    u32x4 of[10];
    SBAR();
    MARK(2, 4);
    {
      u32x4 bv[3];
#pragma unroll
      for (int i = 0; i < 3; ++i) bv[i] = *(const LAS u32x4*)(vp + i * 32);
#pragma unroll
      for (int ks = 0; ks < NPRE; ++ks) { SBAR(); cc = mfma32(w1f[ks], bv[ks % 3], cc); if (ks + 3 < NPRE) bv[ks % 3] = *(const LAS u32x4*)(vp + (ks + 3) * 32);
        if (ks >= OKS && ks < OKS + 10) of[ks - OKS] = ld16s(rs, lane * 16, op + (ks - OKS) * 1024); }
    }
    SBAR();
    const LAS unsigned char* wp = L + L_WO + kh * 51200 + lane * 16;
    u32x4 wa[NWA];
#pragma unroll
    for (int i = 0; i < NWA; ++i) wa[i] = *(const LAS u32x4*)(wp + i * 1024);
    MARK(2, 5);
    RETRY(10, of, lane * 16, op, 1024);
    asm volatile("" :: "v"(st1));
    MARK(2, 6);
    f32x4 acc[5];
#pragma unroll
    for (int cb = 0; cb < 5; ++cb) acc[cb] = f32x4{};
#pragma unroll
    for (int i = 0; i < 50; ++i) {
      acc[i % 5] = mfma16(wa[i % NWA], of[i / 5], acc[i % 5]);
      if (i + NWA < 50) wa[i % NWA] = *(const LAS u32x4*)(wp + (i + NWA) * 1024);
      if (i % 5 == 4 && i < 40) w1f[32 + i / 5] = ld16w(rs, w1v, w1o + (32 + i / 5) * 6912);
      SBAR();
    }
    MARK(2, 7);
    LAS f32x4* red = (LAS f32x4*)(L + L_R3 + tb * 5120) + lane;
    if (kh == 0) { red[2 * 64] = acc[2]; red[3 * 64] = acc[3]; }
    else { red[0] = acc[0]; red[64] = acc[1]; red[4 * 64] = acc[4]; }
    const LAS float* col = (const LAS float*)(L + L_COL);
    float u[12], z[12], ga[12], g2[12];
    const float mux = *(const LAS float*)(L + L_MUX + (tb * 16 + tk) * 4);
#pragma unroll
    for (int q = 0; q < 3; ++q) {
      const int n = q < 2 ? c0 + 4 * q : c1;
      const f32x4 a = *(const LAS f32x4*)(col + n);
#pragma unroll
      for (int e = 0; e < 4; ++e) ga[4 * q + e] = a[e];
    }
    if (kh) { acc[0] = acc[2]; acc[1] = acc[3]; }
    STAGE(12, { const unsigned xw = e < 8 ? xa[e >> 1] : xb[(e - 8) >> 1]; u[e] = (e & 1) ? bfhi(xw) : bflo(xw); });
    STAGE(12, u[e] -= mux; OPQ(u[e]));
    STAGE(12, u[e] = __builtin_fmaf(ga[e], acc[e < 8 ? e >> 2 : 4][e & 3], u[e]); OPQ(u[e]));
#pragma unroll
    for (int q = 0; q < 3; ++q) {
      const f32x4 b = *(const LAS f32x4*)(col + 80 + (q < 2 ? c0 + 4 * q : c1));
#pragma unroll
      for (int e = 0; e < 4; ++e) g2[4 * q + e] = (q == 2 && kh) ? 0.f : b[e];
    }
    MARK(2, 8);
    __syncthreads();
    MARK(2, 9);
    {
      const f32x4 a = red[(2 * kh) * 64], b = red[(2 * kh + 1) * 64], c = red[4 * 64];
      STAGE(12, u[e] = __builtin_fmaf(ga[e], e < 4 ? a[e & 3] : e < 8 ? b[e & 3] : c[e & 3], u[e]); OPQ(u[e]));
    }
    unsigned zu[6];
    STAGE(12, z[e] = u[e] * g2[e]; OPQ(z[e]));
    STAGE(6, zu[e] = pk2(z[2 * e], z[2 * e + 1]));
    STAGE(6, zu[e] = cl(zu[e]); OPQ(zu[e]));
    const int zo = grp + G_Z + (tb >> 1) * 40960 + (tb & 1) * 256 + j * 5120;
    st16s(rs, tk * 16 + g * 512, zo + kh * 2048, u32x4{zu[0], zu[1], zu[2], zu[3]});
    if (!kh) st8s(rs, tk * 16 + (g >> 1) * 512 + (g & 1) * 8, zo + 4096, u32x2{zu[4], zu[5]});
    MARK(2, 10);
    __builtin_amdgcn_s_barrier();
    const int w2o = WS_W2T + (j * 8 + w) * 35840;
#pragma unroll
    for (int i = 0; i < NSE; ++i) w2f[i] = ld16w(rs, lane * 16, w2o + i * 1024);
    {
    float x1[12];
    STAGE(12, x1[e] = u[e] + mux; OPQ(x1[e]));
    *(LAS u32x4*)(L + L_X1 + tid * 16) = u32x4{pk2(x1[0], x1[1]), pk2(x1[2], x1[3]), pk2(x1[4], x1[5]), pk2(x1[6], x1[7])};
    *(LAS u32x2*)(L + L_X1 + 8192 + tid * 8) = u32x2{pk2(x1[8], x1[9]), pk2(x1[10], x1[11])};
    }
    SBAR();
#pragma unroll
    for (int q = 0; q < 3; ++q) {
      const f32x4 a = *(const LAS f32x4*)(col + 160 + (q < 2 ? c0 + 4 * q : c1));
#pragma unroll
      for (int e = 0; e < 4; ++e) ga[4 * q + e] = a[e];
    }
    u32x4 bv[2];
#pragma unroll
    for (int i = 0; i < 2; ++i) bv[i] = *(const LAS u32x4*)(vp + (NPRE + i) * 32);
    if (kh) { u[8] = u[9] = u[10] = u[11] = 0.f; }
    float s[6] = {u[0], u[0] * u[0], z[0], z[0] * g2[0], z[0] * z[0], z[0] * ga[0]};
#pragma unroll
    for (int i = 0; i < 12; ++i) {
      if (NPRE + i < 40) cc = mfma32(w1f[NPRE + i], bv[i & 1], cc);
      if (NPRE + i + 2 < 40) bv[i & 1] = *(const LAS u32x4*)(vp + (NPRE + i + 2) * 32);
      SBAR();
      if (i == 0) continue;
      s[0] += u[i]; s[1] = __builtin_fmaf(u[i], u[i], s[1]); s[2] += z[i]; s[3] = __builtin_fmaf(z[i], g2[i], s[3]); s[4] = __builtin_fmaf(z[i], z[i], s[4]); s[5] = __builtin_fmaf(z[i], ga[i], s[5]);
#pragma unroll
      for (int e = 0; e < 6; ++e) OPQ(s[e]);
      SBAR();
    }
    s[0] = sum16(s[0], s[4]); s[1] = sum16(s[1], s[5]); s[2] = sum16(s[2], s[2]); s[3] = sum16(s[3], s[3]);
#pragma unroll
    for (int e = 0; e < 4; ++e) s[e] = sum32(s[e], s[e]);
    if (g < 2) st16s(rs, lane * 16, grp + G_ST + tb * 8192 + (j * 2 + kh) * 512, u32x4{cl(__builtin_bit_cast(unsigned, s[0])), cl(__builtin_bit_cast(unsigned, s[1])), cl(__builtin_bit_cast(unsigned, s[2])), cl(__builtin_bit_cast(unsigned, s[3]))});
    if (r < 3) {
#pragma unroll
      for (int q = 0; q < 4; ++q) *(LAS f32x4*)(L + L_GSC + w * 384 + r * 128 + q * 32 + h * 16) = f32x4{cc[4 * q], cc[4 * q + 1], cc[4 * q + 2], cc[4 * q + 3]};
    }
    SBAR();
    __builtin_amdgcn_s_barrier();
    MARK(2, 11);
  }
  {
    REIDX(4);
    const int w2o = WS_W2T + (j * 8 + w) * 35840;
    u32x4 zv[10];
    const int zi = grp + G_Z;
#pragma unroll
    for (int i = 0; i < 10; ++i) zv[i] = ld16s(rs, t16, zi + i * 8192);
    SBAR();
#pragma unroll
    for (int i = NSE; i < NS2; ++i) w2f[i] = ld16w(rs, lane * 16, w2o + i * 1024);
    SBAR();
    SBAR();
    MARK(2, 12);
    RETRY(10, zv, t16, zi, 8192);
    MARK(2, 13);
#pragma unroll
    for (int i = 0; i < 10; ++i) *(LAS u32x4*)(L + L_Z + tid * 16 + i * 8192) = zv[i];
    const int so = grp + G_ST + (w & 3) * 8192;
    u32x4 t[8];
    SBAR();
    if (w >= 4) {
#pragma unroll
      for (int i = 0; i < 8; ++i) t[i] = ld16s(rs, lane * 16, so + i * 1024);
    }
    u32x4 ax;
    {
      __builtin_amdgcn_fence(__ATOMIC_RELEASE, "wavefront"); __builtin_amdgcn_wave_barrier(); ORDER();
      const float e0 = *(const LAS float*)(L + L_GSC + w * 384 + h * 256 + r * 4), e1 = *(const LAS float*)(L + (h ? L_B1 + w * 128 : L_GSC + w * 384 + 128) + r * 4);
      const unsigned hh = pk2(e0, e1), ll = pk2(e0 - bflo(hh), e1 - bfhi(hh));
      ax = u32x4{hh, hh, ll, 0u};
      OPQ(ax);
    }
    MARK(2, 14);
    __syncthreads();
    MARK(2, 15);
    STAMP(13);
    MARK(1, 0);
    if (w >= 4) {
      __builtin_amdgcn_s_setprio(3);
      RETRY(8, t, lane * 16, so, 1024);
      f32x4 a = __builtin_bit_cast(f32x4, t[0]);
#pragma unroll
      for (int i = 1; i < 8; ++i) a += __builtin_bit_cast(f32x4, t[i]);
#pragma unroll
      for (int e = 0; e < 4; ++e) a[e] = sum32(a[e], a[e]);
      const float b0 = swap16(a[0]), b1 = swap16(a[1]);
      const f32x4 cst = *(const LAS f32x4*)(L + L_CST); const float css = *(const LAS float*)(L + L_CST + 16);
      const float inv = 1.f / D_;
      const float mu = a[0] * inv;
      const float v1 = fmaxf(a[1] * inv - mu * mu, 0.f) + 1e-5f, r1 = __builtin_amdgcn_rsqf(v1), d1 = v1 * r1;
      const float m2 = r1 * (a[2] - mu * cst[0]) * inv + cst[3] * inv;
      const float e2 = r1 * r1 * (b0 - 2.f * mu * a[3] + mu * mu * cst[1]) * inv + 2.f * r1 * (b1 - mu * cst[2]) * inv + css * inv;
      const float v2 = fmaxf(e2 - m2 * m2, 0.f) + 1e-5f, r2 = __builtin_amdgcn_rsqf(v2);
      const float e0 = h ? -m2 * d1 : -mu, e1 = h ? d1 * v2 * r2 : d1;
      const unsigned hh = pk2(e0, e1), ll = pk2(e0 - bflo(hh), e1 - bfhi(hh));
      if (!(lane & 16)) {
        *(LAS u32x4*)(L + L_SCAL + h * 1024 + ((w - 4) * 16 + (lane & 15)) * 16) = u32x4{hh, ll, hh, 0u};
        *(LAS float*)(L + L_SCAL + 2048 + ((w - 4) * 16 + (lane & 15)) * 4) = r1 * r2;
      }
      __builtin_amdgcn_fence(__ATOMIC_RELEASE, "workgroup");
      *(volatile LAS unsigned*)(L + L_FLG + (w - 4) * 4) = 1u;
      __builtin_amdgcn_s_setprio(PRB);
    } else __builtin_amdgcn_s_setprio(2);
    MARK(1, 1);
    f32x16 a0 = {}, a1 = {};
    const LAS unsigned char* zp = L + L_Z + lane * 16;
    const LAS unsigned char* zq = zp + 40960;
    u32x4 bz[3][2];
#pragma unroll
    for (int i = 0; i < 3; ++i) { bz[i][0] = *(const LAS u32x4*)(zp + i * 1024); bz[i][1] = *(const LAS u32x4*)(zq + i * 1024); }
    SBAR();
#pragma unroll
    for (int ks = 0; ks < 40; ++ks) {
      a0 = mfma32(w1f[ks], bz[ks % 3][0], a0);
      if (ks + 3 < 40) bz[ks % 3][0] = *(const LAS u32x4*)(zp + (ks + 3) * 1024);
      SBAR();
      a1 = mfma32(w1f[ks], bz[ks % 3][1], a1);
      if (ks + 3 < 40) bz[ks % 3][1] = *(const LAS u32x4*)(zq + (ks + 3) * 1024);
      if (ks % 5 != 4 && NS2 + ks - ks / 5 < 35) w2f[NS2 + ks - ks / 5] = ld16w(rs, lane * 16, w2o + (NS2 + ks - ks / 5) * 1024);
      if (ks == 19) MARK(1, 2);
      SBAR();
    }
    if (w < 4) __builtin_amdgcn_s_setprio(PRA); else __builtin_amdgcn_s_setprio(PRB2);
    MARK(1, 3);
    lds_wait<4>(L + L_FLG);
    MARK(1, 4);
    {
      REIDX(6);
      a0 = mfma32(ax, *(const LAS u32x4*)(L + L_SCAL + h * 1024 + r * 16), a0);
      a1 = mfma32(ax, *(const LAS u32x4*)(L + L_SCAL + h * 1024 + 512 + r * 16), a1);
      const float sc0 = *(const LAS float*)(L + L_SCAL + 2048 + r * 4), sc1 = *(const LAS float*)(L + L_SCAL + 2048 + 128 + r * 4);
      const int hv = (r >> 4) * 57344 + (r & 15) * 16, pb = (j & 1) * 14;
#pragma unroll
      for (int tt = 0; tt < 2; ++tt) {
        unsigned o[8];
        MARK(1, 5 + 2 * tt);
        gelu16(tt ? a1 : a0, tt ? sc1 : sc0, o);
        MARK(1, 6 + 2 * tt);
        const int hs = grp + G_HH + tt * 2 * 57344 + ((w & 4) + (j >> 1)) * 7168;
        st16s(rs, hv + h * 256, hs + (pb + 3 * (w & 3)) * 256, u32x4{o[0], o[1], o[2], o[3]});
        if (!h) st16s(rs, hv, hs + (pb + 3 * (w & 3) + 2) * 256, u32x4{o[4], o[5], o[6], o[7]});
        else st8s(rs, hv, hs + (pb + 12 + ((w & 3) >> 1)) * 256 + (w & 1) * 8, u32x2{o[4], o[5]});
        SBAR();
      }
    }
    __builtin_amdgcn_s_setprio(0);
    MARK(1, 9);
  }
  {
    REIDX(5);
    const int hp = grp + G_HH + w * 7168;
    LAS f32x4* red = (LAS f32x4*)L;
    f32x4 keep[3] = {f32x4{}, f32x4{}, f32x4{}};
    u32x4 hf[14];
#define HOFF(i) (((i) >= 7 ? 57344 - 7168 : 0) + (i) * 1024)
    if (w < 4) __builtin_amdgcn_s_sleep(ASLP);
#pragma unroll
    for (int i = 0; i < 14; ++i) hf[i] = ld16s(rs, lane * 16, hp + HOFF(i));
    if (w >= 4) __syncthreads();
    RETRYF(14, hf, lane * 16, hp + HOFF(i_));
    if (w < 4) __syncthreads();
#pragma unroll
    for (int tq = 0; tq < 4; ++tq) {
      u32x4* hq = hf + (tq & 1) * 7;
      if (tq >= 2) RETRY(7, hq, lane * 16, hp + tq * 57344, 1024);
      MARK(1, 10 + tq);
      f32x4 acc[5];
#pragma unroll
      for (int cb = 0; cb < 5; ++cb) acc[cb] = f32x4{};
#pragma unroll
      for (int ks = 0; ks < 7; ++ks) {
#pragma unroll
        for (int cb = 0; cb < 5; ++cb) acc[cb] = mfma16(w2f[ks * 5 + cb], hq[ks], acc[cb]);
        if (tq < 2) hq[ks] = ld16s(rs, lane * 16, hp + (tq + 2) * 57344 + ks * 1024);
        SBAR();
      }
#pragma unroll
      for (int cb = 0; cb < 5; ++cb) {
        const int dst = tq + ((cb == 2 || cb == 3) ? 4 : 0);
        if (dst == w) keep[cb == 4 ? 2 : (cb & 1)] = acc[cb];
        else red[RSLOT(tq * 5 + cb, (w - dst - 1) & 7) * 64 + lane] = acc[cb];
      }
    }
    const u32x4 x1a = *(const LAS u32x4*)(L + L_X1 + tid * 16); const u32x2 x1b = *(const LAS u32x2*)(L + L_X1 + 8192 + tid * 8);
    MARK(1, 14);
    __syncthreads();
    MARK(1, 15);
    STAMP(17);
    const LAS float* col = (const LAS float*)(L + L_COL);
    const int g = lane >> 4, tok3 = tg * 64 + tb * 16 + (lane & 15), c0 = kh * 32 + g * 8, c1 = 64 + g * 4;
    float v[12];
#pragma unroll
    for (int q = 0; q < 3; ++q) {
      const int cb = q == 2 ? 4 : 2 * kh + q;
      f32x4 s = keep[q];
#pragma unroll
      for (int i = 0; i < 7; ++i) s += red[RSLOT(tb * 5 + cb, i) * 64 + lane];
      const int n = q < 2 ? c0 + 4 * q : c1;
      const f32x4 gb = *(const LAS f32x4*)(col + 240 + n), b2 = *(const LAS f32x4*)(col + 320 + n);
#pragma unroll
      for (int e = 0; e < 4; ++e) {
        const unsigned xw = q < 2 ? x1a[2 * q + (e >> 1)] : x1b[e >> 1];
        const float xx = (e & 1) ? bfhi(xw) : bflo(xw);
        v[4 * q + e] = xx + gb[e] * (s[e] + b2[e]);
      }
    }
    uint16_t* op = p.out + (size_t)tok3 * D_ + j * 80;
    *(u32x4*)(op + c0) = u32x4{pk2(v[0], v[1]), pk2(v[2], v[3]), pk2(v[4], v[5]), pk2(v[6], v[7])};
    if (!kh) *(u32x2*)(op + c1) = u32x2{pk2(v[8], v[9]), pk2(v[10], v[11])};
    if (blockIdx.x == 0 && tid == 0) stflag((unsigned*)(p.ws + WS_EPO), tag);
    STAMP(18);
    STAMP(19);
  }
}

extern "C" void kernel_launch(void* const* d_in, const int* in_sizes, int n_in, void* d_out, int out_size, void* d_ws, size_t ws_size, hipStream_t stream) {
  Params p;
  p.x = (const uint16_t*)d_in[0]; p.wq = (const uint16_t*)d_in[1]; p.wk = (const uint16_t*)d_in[2]; p.wv = (const uint16_t*)d_in[3]; p.wo = (const uint16_t*)d_in[4];
  p.w1 = (const uint16_t*)d_in[6]; p.b1 = (const uint16_t*)d_in[7]; p.w2 = (const uint16_t*)d_in[8]; p.b2 = (const uint16_t*)d_in[9];
  p.c = (const uint16_t*)d_in[10]; p.wmod = (const uint16_t*)d_in[11];
  p.out = (uint16_t*)d_out; p.ws = (unsigned char*)d_ws;
  (void)hipFuncSetAttribute((const void*)k_mega, hipFuncAttributeMaxDynamicSharedMemorySize, LDS_BYTES);
  k_mega<<<256, 512, LDS_BYTES, stream>>>(p);
}
```
